```python
import torch

def _softmax(x, axis=-1):
    return torch.softmax(x, dim=axis)
def _gelu(x):
    return torch.nn.functional.gelu(x, approximate="tanh")
def _silu(x):
    return torch.nn.functional.silu(x)
def _sigmoid(x):
    return torch.sigmoid(x)
def _layernorm(x, axis=-1, eps=1e-5):
    m = x.mean(dim=axis, keepdim=True)
    v = x.var(dim=axis, keepdim=True, unbiased=False)
    return (x - m) / torch.sqrt(v + eps)
def _scatter_add(tgt, idx, src, axis=0):
    return tgt.clone().index_add_(axis, idx, src)
def _segment_sum(src, seg, n):
    out = torch.zeros((n,) + src.shape[1:], dtype=src.dtype)
    return out.index_add_(0, seg, src)
def _pad(x, pw):
    flat = tuple(v for pair in reversed(pw) for v in pair)
    return torch.nn.functional.pad(x, flat)

kernel_name = 'concat_heads__ssm_conv_s4__softmax_full_mha__prompt'

def setup_inputs():

    _w = (lambda a: (lambda t: t.to(torch.bfloat16) if t.is_floating_point() else t)(torch.as_tensor(a)))
    def _A_setup():
        B, L, D, N = 1, 2048, 576, 128
        A_re = -(lambda _x: torch.exp(torch.as_tensor(_x)))((lambda *s: torch.randn(*s, dtype=torch.bfloat16) * 6.4)(D, N))
        A_im = torch.pi * (lambda *s: torch.randn(*s, dtype=torch.bfloat16) * 0.1)(D, N)
        B_re = (lambda *s: torch.randn(*s, dtype=torch.bfloat16) * 0.2)(D, N)
        B_im = (lambda *s: torch.randn(*s, dtype=torch.bfloat16) * 0.2)(D, N)
        C_re = (lambda *s: torch.randn(*s, dtype=torch.bfloat16) * 0.2)(D, N)
        C_im = (lambda *s: torch.randn(*s, dtype=torch.bfloat16) * 0.2)(D, N)
        log_dt = (lambda *s: torch.randn(*s, dtype=torch.bfloat16) * 0.1)(D) * 0.5 - 4.0
        Dskip = (lambda *s: torch.randn(*s, dtype=torch.bfloat16) * 0.1)(D)
        return {
            "u": _w((lambda *s: torch.randn(*s, dtype=torch.bfloat16) * 0.05)(B, L, D)),
            "A_re": _w(A_re),
            "A_im": _w(A_im),
            "B_re": _w(B_re),
            "B_im": _w(B_im),
            "C_re": _w(C_re),
            "C_im": _w(C_im),
            "log_dt": _w(log_dt),
            "Dskip": _w(Dskip),
        }
    _A_in = _A_setup()

    def _B_setup():
        B, S, D = 1, 2048, 576
        return {
            "x": _w((lambda *s: torch.randn(*s, dtype=torch.bfloat16) * 0.2)(B, S, D)),
            "wqkv": _w((lambda *s: torch.randn(*s, dtype=torch.bfloat16) * 0.2)(D, 3 * D)),
            "wo": _w((lambda *s: torch.randn(*s, dtype=torch.bfloat16) * 0.05)(D, D)),
        }
    _B_in = _B_setup()

    return {
        "A_u": _A_in["u"],
        "A_A_re": _A_in["A_re"],
        "A_A_im": _A_in["A_im"],
        "A_B_re": _A_in["B_re"],
        "A_B_im": _A_in["B_im"],
        "A_C_re": _A_in["C_re"],
        "A_C_im": _A_in["C_im"],
        "A_log_dt": _A_in["log_dt"],
        "A_Dskip": _A_in["Dskip"],
        "B_x": _B_in["x"],
        "B_wqkv": _B_in["wqkv"],
        "B_wo": _B_in["wo"],
    }

def reference(A_u, A_A_re, A_A_im, A_B_re, A_B_im, A_C_re, A_C_im, A_log_dt, A_Dskip, B_x, B_wqkv, B_wo):
    def _A_ref(u, A_re, A_im, B_re, B_im, C_re, C_im, log_dt, Dskip):
        Bsz, L, D = u.shape
        dt = torch.exp(log_dt)[:, None]
        A = A_re + 1j * A_im
        Bc = B_re + 1j * B_im
        Cc = C_re + 1j * C_im
        logdA = dt * A
        dB = dt * Bc
        l = (lambda x, dt: x.to(dt))(torch.arange(L), A_re.dtype)
        P = torch.exp(logdA[:, :, None] * l[None, None, :])
        K = torch.einsum("dn,dnl,dn->dl", Cc, P, dB).real
        L2 = 2 * L
        Kp = _pad(K, ((0, 0), (0, L)))
        up = (lambda x, *d: x.permute(*d))(u, 0, 2, 1)
        up = _pad(up, ((0, 0), (0, 0), (0, L)))
        Kf = (lambda x, axes=(-2,-1): torch.fft.rfft2(x.float(), dim=axes))(Kp[:, None, :], axes=(-2, -1))
        Uf = (lambda x, axes=(-2,-1): torch.fft.rfft2(x.float(), dim=axes))(up[:, :, None, :], axes=(-2, -1))
        Yf = Uf * Kf[None, :, :, :]
        y = (lambda x, s, axes=(-2,-1): torch.fft.irfft2(x, s=s, dim=axes).to(torch.bfloat16))(Yf, (1, L2), axes=(-2, -1))[:, :, 0, :L]
        y = (lambda x, *d: x.permute(*d))(y, 0, 2, 1) + u * Dskip[None, None, :]
        return y

    def _B_ref(x, wqkv, wo):
        B, S, D = x.shape
        H = 32; Dh = D // H
        qkv = (lambda x, *d: x.permute(*d))((x @ wqkv).reshape(B, S, 3, H, Dh), 2, 0, 3, 1, 4)
        q, k, v = qkv[0], qkv[1], qkv[2]
        scale = Dh ** -0.5
        scores = torch.einsum("bhqd,bhkd->bhqk", q, k) * scale
        p = _softmax(scores, axis=-1)
        o = torch.einsum("bhqk,bhkd->bhqd", p, v)
        return (lambda x, *d: x.permute(*d))(o, 0, 2, 1, 3).reshape(B, S, D) @ wo

    a = _A_ref(A_u, A_A_re, A_A_im, A_B_re, A_B_im, A_C_re, A_C_im, A_log_dt, A_Dskip)
    b = _B_ref(B_x, B_wqkv, B_wo)
    return torch.concatenate([a, b], axis=-1)
```

```cpp
#include <hip/hip_runtime.h>
#include <hip/hip_bf16.h>
#include <stdint.h>

typedef unsigned short u16;
__device__ __forceinline__ float bf2f(u16 b) { return __uint_as_float(((unsigned)b) << 16); }
__device__ __forceinline__ u16 f2bf(float f) { unsigned u = __float_as_uint(f); u += 0x7FFFu + ((u >> 16) & 1u); return (u16)(u >> 16); }
__device__ __forceinline__ float rbf(float f) { return bf2f(f2bf(f)); }

constexpr int L = 2048, D = 576, N = 128, H = 32, DH = 18;

__global__ void k_qkv(const u16* x, const u16* w, u16* qkv) {
    int col = blockIdx.x * 64 + (threadIdx.x & 63);
    int row = blockIdx.y * 4 + (threadIdx.x >> 6);
    float acc = 0;
    for (int k = 0; k < D; k++) acc += bf2f(x[row * D + k]) * bf2f(w[k * 3 * D + col]);
    qkv[row * 3 * D + col] = f2bf(acc);
}
__global__ void k_attn(const u16* qkv, u16* o) {
    int h = blockIdx.y;
    int q = blockIdx.x * 64 + threadIdx.x;
    float qv[DH];
    for (int i = 0; i < DH; i++) qv[i] = bf2f(qkv[q * 3 * D + h * DH + i]);
    float m = -1e30f, l = 0; float acc[DH];
    for (int i = 0; i < DH; i++) acc[i] = 0;
    const float scale = 0.23570226039551584f;
    for (int k = 0; k < L; k++) {
        float s = 0;
        for (int i = 0; i < DH; i++) s += qv[i] * bf2f(qkv[k * 3 * D + D + h * DH + i]);
        s *= scale;
        float mn = fmaxf(m, s);
        float a = __expf(m - mn), p = __expf(s - mn);
        l = l * a + p;
        for (int i = 0; i < DH; i++) acc[i] = acc[i] * a + p * bf2f(qkv[k * 3 * D + 2 * D + h * DH + i]);
        m = mn;
    }
    for (int i = 0; i < DH; i++) o[q * D + h * DH + i] = f2bf(acc[i] / l);
}
__global__ void k_oproj(const u16* o, const u16* wo, u16* out) {
    int col = blockIdx.x * 64 + (threadIdx.x & 63);
    int row = blockIdx.y * 4 + (threadIdx.x >> 6);
    float acc = 0;
    for (int k = 0; k < D; k++) acc += bf2f(o[row * D + k]) * bf2f(wo[k * D + col]);
    out[row * 2 * D + D + col] = f2bf(acc);
}
__global__ void k_kgen(const u16* Are, const u16* Aim, const u16* Bre, const u16* Bim, const u16* Cre, const u16* Cim, const u16* logdt, float* K) {
    int d = blockIdx.y;
    int l = blockIdx.x * 256 + threadIdx.x;
    float dt = rbf(expf(bf2f(logdt[d])));
    float lb = rbf((float)l);
    float acc = 0;
    for (int n = 0; n < N; n++) {
        float ar = dt * bf2f(Are[d * N + n]), ai = dt * bf2f(Aim[d * N + n]);
        float br = dt * bf2f(Bre[d * N + n]), bi = dt * bf2f(Bim[d * N + n]);
        float cr = bf2f(Cre[d * N + n]), ci = bf2f(Cim[d * N + n]);
        float wr = cr * br - ci * bi, wi = cr * bi + ci * br;
        float e = expf(ar * lb);
        float s, c; sincosf(ai * lb, &s, &c);
        acc += e * (wr * c - wi * s);
    }
    K[d * L + l] = acc;
}
__global__ void k_conv(const u16* u, const float* K, const u16* Dskip, u16* out) {
    int d = blockIdx.x * 64 + (threadIdx.x & 63);
    int t = blockIdx.y * 4 + (threadIdx.x >> 6);
    float acc = 0;
    for (int j = 0; j <= t; j++) acc += K[d * L + j] * bf2f(u[(t - j) * D + d]);
    float y = rbf(acc);
    float sk = rbf(bf2f(u[t * D + d]) * bf2f(Dskip[d]));
    out[t * 2 * D + d] = f2bf(y + sk);
}

extern "C" void kernel_launch(void* const* d_in, const int* in_sizes, int n_in,
                              void* d_out, int out_size, void* d_ws, size_t ws_size,
                              hipStream_t stream) {
    const u16* u = (const u16*)d_in[0];
    const u16* Are = (const u16*)d_in[1]; const u16* Aim = (const u16*)d_in[2];
    const u16* Bre = (const u16*)d_in[3]; const u16* Bim = (const u16*)d_in[4];
    const u16* Cre = (const u16*)d_in[5]; const u16* Cim = (const u16*)d_in[6];
    const u16* logdt = (const u16*)d_in[7]; const u16* Dskip = (const u16*)d_in[8];
    const u16* x = (const u16*)d_in[9]; const u16* wqkv = (const u16*)d_in[10]; const u16* wo = (const u16*)d_in[11];
    u16* out = (u16*)d_out;
    char* ws = (char*)d_ws;
    u16* qkv = (u16*)ws;
    u16* o = (u16*)(ws + (8 << 20));
    float* K = (float*)(ws + (12 << 20));
    k_qkv<<<dim3(27, 512), 256, 0, stream>>>(x, wqkv, qkv);
    k_attn<<<dim3(32, 32), 64, 0, stream>>>(qkv, o);
    k_oproj<<<dim3(9, 512), 256, 0, stream>>>(o, wo, out);
    k_kgen<<<dim3(8, 576), 256, 0, stream>>>(Are, Aim, Bre, Bim, Cre, Cim, logdt, K);
    k_conv<<<dim3(9, 512), 256, 0, stream>>>(u, K, Dskip, out);
}
```

```cpp
#include <hip/hip_runtime.h>
#include <stdint.h>
typedef unsigned short u16;
typedef __attribute__((ext_vector_type(8))) short bf16x8;
typedef __attribute__((ext_vector_type(4))) short bf16x4;
typedef __attribute__((ext_vector_type(16))) float f32x16;
typedef __attribute__((ext_vector_type(4))) float f32x4;
typedef __attribute__((ext_vector_type(2))) float f32x2;
typedef __attribute__((ext_vector_type(2))) __bf16 bf2;
typedef __attribute__((ext_vector_type(4))) unsigned u32x4;
typedef __attribute__((ext_vector_type(2))) unsigned u32x2;
#define LDS __attribute__((address_space(3)))
#define MFMA32(a,b,c) __builtin_amdgcn_mfma_f32_32x32x16_bf16(a,b,c,0,0,0)
#define MFMA16(a,b,c) __builtin_amdgcn_mfma_f32_16x16x32_bf16(a,b,c,0,0,0)
__device__ __forceinline__ unsigned pk(float a, float b) {
    f32x2 v = {a, b};
    bf2 r = __builtin_convertvector(v, bf2);
    return __builtin_bit_cast(unsigned, r);
}
__device__ __forceinline__ float bf2f(u16 b) { return __uint_as_float(((unsigned)b) << 16); }
__device__ __forceinline__ void glds16s(unsigned voff, const void* sbase, unsigned lds_base) {
    unsigned sv; asm volatile("s_mov_b32 %0, m0\n\ts_mov_b32 m0, %3\n\ts_nop 0\n\tglobal_load_lds_dwordx4 %1, %2\n\ts_mov_b32 m0, %0" : "=&s"(sv) : "v"(voff), "s"(sbase), "s"(lds_base) : "memory"); }
__device__ __forceinline__ void glds_x4(unsigned voff, const void* sbase, unsigned lds_base) {
    unsigned sv; asm volatile("s_mov_b32 %0, m0\n\ts_mov_b32 m0, %3\n\ts_nop 0\n\tglobal_load_lds_dwordx4 %1, %2\n\tglobal_load_lds_dwordx4 %1, %2 offset:1024\n\t"
        "global_load_lds_dwordx4 %1, %2 offset:2048\n\tglobal_load_lds_dwordx4 %1, %2 offset:3072\n\ts_mov_b32 m0, %0" : "=&s"(sv) : "v"(voff), "s"(sbase), "s"(lds_base) : "memory"); }
__device__ __forceinline__ bf16x4 tr4(LDS char* p) { typedef short v4i16_t __attribute__((ext_vector_type(4)));
    return __builtin_bit_cast(bf16x4, __builtin_amdgcn_ds_read_tr16_b64_v4i16((LDS v4i16_t*)p)); }
__device__ __forceinline__ bf16x8 cat8(bf16x4 a, bf16x4 b) { return __builtin_shufflevector(a, b, 0, 1, 2, 3, 4, 5, 6, 7); }

constexpr int NS = 128;
constexpr float QSCALE = 0.23570226039551584f * 1.4426950408889634f;

__device__ const unsigned QK_TAB[288] = {
  5111809,5111811,0,0,0,0,5911041,6070785,
  5112321,5271553,5431297,5431299,0,0,0,0,
  0,5272065,5431809,5591041,5750785,5750787,0,0,
  0,0,0,5591553,5751297,5910529,6070273,6070275,
  6230017,6389761,6389763,0,0,0,0,7188993,
  6230529,6390273,6549505,6709249,6709251,0,0,0,
  0,0,6550017,6709761,6868993,7028737,7028739,0,
  0,0,0,0,6869505,7029249,7188481,7348226,
  7348737,7507969,7667713,7667715,0,0,0,0,
  7348234,7508481,7668225,7827457,7987201,7987203,0,0,
  0,0,0,7827969,7987713,8146945,8306689,8306691,
  7348227,0,0,0,0,8147457,8307201,8466434,
  8466945,8626689,8785921,8945665,8945667,0,0,0,
  8466442,0,8786433,8946177,9105409,9265153,9265155,0,
  0,0,0,0,9105921,9265665,9424897,9584642,
  8626177,8626179,0,0,0,0,9425409,9585154,
  9584650,9744897,9904641,10063873,1,3,0,0,
  9585162,0,0,10064385,513,159745,319489,319491,
  9584643,0,0,0,0,160257,320001,479234,
  9744385,9904129,9904131,0,0,0,0,479746,
  479242,0,799233,958977,1118209,1277953,1277955,0,
  479754,0,0,0,1118721,1278465,1437697,1597441,
  638977,638979,0,0,0,0,1438209,1597954,
  639489,798721,958465,958467,0,0,0,0,
  1597962,0,0,2077185,2236929,2396161,2555905,2555907,
  1597443,0,0,0,0,2396673,2556417,2715649,
  1757185,1916929,1916931,0,0,0,0,2716162,
  1757697,1917441,2076673,2236417,2236419,0,0,0,
  2716170,0,0,0,3355137,3514881,3674113,3833857,
  2875393,2875395,0,0,0,0,3674625,3834369,
  2875905,3035137,3194881,3194883,0,0,0,0,
  0,3035649,3195393,3354625,3514369,3514371,0,0,
  3833859,0,0,0,0,4633089,4792833,4952065,
  3993601,4153345,4153347,0,0,0,0,4952577,
  3994113,4153857,4313089,4472833,4472835,0,0,0,
  0,0,4313601,4473345,4632577,4792321,4792323,0,
};
constexpr int G1_SLOT = 16384;
constexpr int G1_NSLOT = 7, G1_INIT = 4;
constexpr int G1_RING = G1_NSLOT * G1_SLOT;
constexpr int KV_T = 2496;
constexpr int KV_H = 64 * KV_T;
constexpr int CT_OFF = 4 * 16384;
constexpr int CT_ROW = 264;
__device__ __forceinline__ void K1ST16(char* b, unsigned o, u32x4 v) { *(u32x4*)(b + o) = v; }
__device__ __forceinline__ void K1ST8(char* b, unsigned o, u32x2 v) { *(u32x2*)(b + o) = v; }
__device__ __forceinline__ void st16_sc1(char* base, unsigned off, u32x4 v) { asm volatile("global_store_dwordx4 %0, %1, %2 sc1\n\ts_nop 1" :: "v"(off), "v"(v), "s"(base) : "memory"); }
__device__ __forceinline__ void st8_sc1(char* base, unsigned off, u32x2 v) { asm volatile("global_store_dwordx2 %0, %1, %2 sc1\n\ts_nop 1" :: "v"(off), "v"(v), "s"(base) : "memory"); }
__device__ __forceinline__ void st4_sc1(char* base, unsigned off, unsigned v) { asm volatile("global_store_dword %0, %1, %2 sc1\n\ts_nop 1" :: "v"(off), "v"(v), "s"(base) : "memory"); }


struct QkTab { unsigned e[8]; };
__device__ __forceinline__ QkTab qk_tab(int cb, int w) { QkTab t; const unsigned* tab = QK_TAB + (cb * 4 + (w >> 1)) * 8;
    #pragma unroll
    for (int i = 0; i < 8; i++) t.e[i] = tab[i];
    return t; }
__device__ __forceinline__ void qk_scatter(char* __restrict__ vimg, LDS char* sm, const QkTab& T, int tt, int w, int lane) {
    const int th = w & 1, token = th * 64 + lane;
    LDS char* src = sm + CT_OFF + token * CT_ROW + (w >> 1) * 8;
    u32x2 v[8], v2[8];
    #pragma unroll
    for (int i = 0; i < 8; i++) { v[i] = *(LDS u32x2*)(src + i * 32); v2[i] = *(LDS u32x2*)(src + i * 32 + 16); }
    const unsigned lo = (tt * 4 + (token >> 5)) * KV_T + (lane & 31) * 16, lt = (tt * 4 + (token >> 5)) * KV_T + 1024 + (lane & 31) * 8;
    const unsigned* e = T.e;
    #pragma unroll
    for (int i = 0; i < 8; i++) {
        const unsigned kind = e[i] & 3u;
        char* rec = vimg + (e[i] & ~7u);
        if (kind == 1) K1ST16(rec, lo, (u32x4){v[i][0], v[i][1], v2[i][0], v2[i][1]});
        else if (kind == 2) K1ST8(vimg + (e[i] & ~3u), lo, v[i]);
        else if (kind == 3) { K1ST8(rec, lt, v[i]); K1ST8(rec + KV_H, lt, v[i]); }
    }
}
template<bool isv>
__device__ __forceinline__ void qkv_consume(char* __restrict__ vimg, char* smem, int cb, int tt, int w, int lane) {
    LDS char* sm = (LDS char*)smem;
    const int wr = w & 1, wt = w >> 1;
    const int r32 = lane & 31, h = lane >> 5, blk = (lane >> 4) & 1, q4 = (lane & 15) >> 2, p4 = lane & 3;
    unsigned aX[2][4], aW[2];
    #pragma unroll
    for (int t2 = 0; t2 < 2; t2++) { const int row = wt * 64 + t2 * 32 + r32;
        #pragma unroll
        for (int ks = 0; ks < 4; ks++) aX[t2][ks] = row * 128 + (((2 * ks + h) ^ ((row >> 1) & 7)) * 16); }
    #pragma unroll
    for (int i = 0; i < 2; i++) { const int chunk = wr * 8 + i * 4 + 2 * blk + (p4 >> 1);
        aW[i] = (8 * h + q4) * 256 + ((chunk ^ (q4 << 2)) * 16) + (p4 & 1) * 8; }
    f32x16 acc[2][2];
    #pragma unroll
    for (int i = 0; i < 16; i++) { acc[0][0][i] = 0; acc[0][1][i] = 0; acc[1][0][i] = 0; acc[1][1][i] = 0; }
    bf16x8 x0[2], x1[2], w0[2], w1[2];
    #define G1_LOAD(B, SX, SW, KS) do { x0[B] = *(LDS bf16x8*)(sm + (SX) + aX[0][KS]); x1[B] = *(LDS bf16x8*)(sm + (SX) + aX[0][KS] + 4096); \
        w0[B] = cat8(tr4(sm + (SW) + aW[0] + (KS) * 4096), tr4(sm + (SW) + aW[0] + (KS) * 4096 + 1024)); \
        w1[B] = cat8(tr4(sm + (SW) + aW[1] + (KS) * 4096), tr4(sm + (SW) + aW[1] + (KS) * 4096 + 1024)); } while (0)
    #define G1_MMA(C) do { if constexpr (!isv) { acc[0][0] = MFMA32(w0[C], x0[C], acc[0][0]); acc[0][1] = MFMA32(w0[C], x1[C], acc[0][1]); acc[1][0] = MFMA32(w1[C], x0[C], acc[1][0]); acc[1][1] = MFMA32(w1[C], x1[C], acc[1][1]); } \
        else { acc[0][0] = MFMA32(x0[C], w0[C], acc[0][0]); acc[0][1] = MFMA32(x1[C], w0[C], acc[0][1]); acc[1][0] = MFMA32(x0[C], w1[C], acc[1][0]); acc[1][1] = MFMA32(x1[C], w1[C], acc[1][1]); } } while (0)
    asm volatile("s_barrier" ::: "memory");
    int sx = 0, sw = G1_SLOT;
    G1_LOAD(0, sx, sw, 0);
    #define G1_HEAD() do { G1_LOAD(1, sx, sw, 1); __builtin_amdgcn_sched_barrier(0); G1_MMA(0); __builtin_amdgcn_sched_barrier(0); \
        G1_LOAD(0, sx, sw, 2); __builtin_amdgcn_sched_barrier(0); G1_MMA(1); __builtin_amdgcn_sched_barrier(0); \
        G1_LOAD(1, sx, sw, 3); __builtin_amdgcn_sched_barrier(0); G1_MMA(0); __builtin_amdgcn_sched_barrier(0); } while (0)
    #pragma unroll 2
    for (int s = 0; s < 8; s++) {
        G1_HEAD();
        asm volatile("s_waitcnt lgkmcnt(0)\n\ts_barrier" ::: "memory");
        sx += 2 * G1_SLOT; if (sx >= G1_RING) sx -= G1_RING;
        sw += 2 * G1_SLOT; if (sw >= G1_RING) sw -= G1_RING;
        G1_LOAD(0, sx, sw, 0);
        __builtin_amdgcn_sched_barrier(0); G1_MMA(1); __builtin_amdgcn_sched_barrier(0);
    }
    G1_HEAD();
    G1_MMA(1);
    if constexpr (!isv) {
        const QkTab T = qk_tab(cb, w);
        const int G0 = 32 * cb + 16 * wr + h;
        LDS char* cw = sm + CT_OFF + (wt * 64 + r32) * CT_ROW + (16 * wr + h) * 8;
        #pragma unroll
        for (int k = 0; k < 8; k++) {
            const float sc = (G0 + 2 * k) < 144 ? QSCALE : 1.0f;
            #pragma unroll
            for (int j = 0; j < 2; j++) {
                const f32x16& a = acc[k >> 2][j]; const int g = k & 3;
                *(LDS u32x2*)(cw + j * 32 * CT_ROW + k * 16) = (u32x2){pk(a[4 * g] * sc, a[4 * g + 1] * sc), pk(a[4 * g + 2] * sc, a[4 * g + 3] * sc)};
            }
        }
        asm volatile("s_waitcnt lgkmcnt(0)\n\ts_barrier" ::: "memory");
        qk_scatter(vimg, sm, T, tt, w, lane);
    } else {
        const int tile0 = tt * 4 + wt * 2;
        #pragma unroll
        for (int i = 0; i < 2; i++) {
            const int col = 128 * (cb - 9) + 64 * wr + 32 * i + r32;
            if (col < 576) {
                const int head = (col * 3641) >> 16, dim = col - 18 * head;
                const unsigned ob = (unsigned)head * KV_H + tile0 * KV_T + 1280 + (h * 19 + dim) * 16;
                #pragma unroll
                for (int j = 0; j < 2; j++) {
                    u32x4 v0, v1;
                    #pragma unroll
                    for (int e = 0; e < 4; e++) { v0[e] = pk(acc[i][j][2 * e], acc[i][j][2 * e + 1]); v1[e] = pk(acc[i][j][8 + 2 * e], acc[i][j][8 + 2 * e + 1]); }
                    const unsigned d = ob + j * KV_T;
                    K1ST16(vimg, d, v0); K1ST16(vimg, d + 608, v1);
                    if (dim == 17) { K1ST16(vimg, d + 16, (u32x4)0x3F803F80u); K1ST16(vimg, d + 608 + 16, (u32x4)0x3F803F80u); }
                }
            }
        }
    }
}

__device__ __forceinline__ void qkv_load(const u16* __restrict__ x, const u16* __restrict__ wqkv, char* __restrict__ vimg, char* smem, int cb, int tt, int j, int lane) {
    const unsigned lds0 = (unsigned)(uintptr_t)(LDS char*)smem;
    unsigned vx[4], vw[4];
    #pragma unroll
    for (int i = 0; i < 4; i++) {
        const int ins = j * 4 + i;
        { const int row = ins * 8 + (lane >> 3), c = (lane & 7) ^ ((row >> 1) & 7); vx[i] = (tt * 128 + row) * 1152 + c * 16; }
        { const int row = ins * 4 + (lane >> 4); int c = (lane & 15) ^ ((row & 3) << 2); if (cb == 13) c &= 7; vw[i] = row * 3456 + cb * 256 + c * 16; }
    }
    static_assert(G1_NSLOT == 7 && G1_INIT == 4, "the counted waits below are written for this ring");
    const char* xb = (const char*)x; const char* wb = (const char*)wqkv;
    unsigned slot = lds0 + j * 4096;
    const unsigned ring_end = lds0 + G1_RING + j * 4096;
    #define G1_PX() do { _Pragma("unroll") for (int i = 0; i < 4; i++) glds16s(vx[i], xb, slot + i * 1024); xb += 128; slot += G1_SLOT; if (slot >= ring_end) slot -= G1_RING; } while (0)
    #define G1_PW() do { _Pragma("unroll") for (int i = 0; i < 4; i++) glds16s(vw[i], wb, slot + i * 1024); wb += 64 * 3456; slot += G1_SLOT; if (slot >= ring_end) slot -= G1_RING; } while (0)
    G1_PX(); G1_PW(); G1_PX(); G1_PW();
    asm volatile("s_waitcnt vmcnt(8)\n\ts_barrier" ::: "memory");
    G1_PX(); G1_PW(); G1_PX();
    #pragma unroll 1
    for (int s = 1; s <= 5; s++) {
        asm volatile("s_waitcnt vmcnt(12)\n\ts_barrier" ::: "memory");
        G1_PW(); G1_PX();
    }
    asm volatile("s_waitcnt vmcnt(12)\n\ts_barrier" ::: "memory");
    G1_PW();
    asm volatile("s_waitcnt vmcnt(8)\n\ts_barrier" ::: "memory");
    asm volatile("s_waitcnt vmcnt(0)\n\ts_barrier" ::: "memory");
    #undef G1_PX
    #undef G1_PW
    if (cb < 9) {
        const QkTab T = qk_tab(cb, 4 + j);
        asm volatile("s_waitcnt lgkmcnt(0)\n\ts_barrier" ::: "memory");
        qk_scatter(vimg, (LDS char*)smem, T, tt, 4 + j, lane);
    }
}
__device__ const unsigned short KEXP_TAB[4096] = {
  2556,2556,2556,2556,2552,2552,2552,2552,2552,0,0,0,0,0,0,0,
  2552,2552,2552,2548,2548,2548,2548,2548,2548,0,0,0,0,0,0,0,
  2548,2548,2548,2548,2544,2544,2544,2544,2544,0,0,0,0,0,0,0,
  2544,2544,2544,2540,2540,2540,2540,2540,2540,0,0,0,0,0,0,0,
  2540,2540,2540,2540,2536,2536,2536,2536,2536,0,0,0,0,0,0,0,
  2536,2536,2536,2532,2532,2532,2532,2532,2532,0,0,0,0,0,0,0,
  2532,2532,2532,2532,2528,2528,2528,2528,2528,0,0,0,0,0,0,0,
  2528,2528,2528,2524,2524,2524,2524,2524,2524,0,0,0,0,0,0,0,
  2524,2524,2524,2524,2520,2520,2520,2520,2520,0,0,0,0,0,0,0,
  2520,2520,2520,2516,2516,2516,2516,2516,2516,0,0,0,0,0,0,0,
  2516,2516,2516,2516,2512,2512,2512,2512,2512,0,0,0,0,0,0,0,
  2512,2512,2512,2508,2508,2508,2508,2508,2508,0,0,0,0,0,0,0,
  2508,2508,2508,2508,2504,2504,2504,2504,2504,0,0,0,0,0,0,0,
  2504,2504,2504,2500,2500,2500,2500,2500,2500,0,0,0,0,0,0,0,
  2500,2500,2500,2500,2496,2496,2496,2496,2496,0,0,0,0,0,0,0,
  2496,2496,2496,2492,2492,2492,2492,2492,2492,0,0,0,0,0,0,0,
  2492,2492,2492,2492,2488,2488,2488,2488,2488,0,0,0,0,0,0,0,
  2488,2488,2488,2484,2484,2484,2484,2484,2484,0,0,0,0,0,0,0,
  2484,2484,2484,2484,2480,2480,2480,2480,2480,0,0,0,0,0,0,0,
  2480,2480,2480,2476,2476,2476,2476,2476,2476,0,0,0,0,0,0,0,
  2476,2476,2476,2476,2472,2472,2472,2472,2472,0,0,0,0,0,0,0,
  2472,2472,2472,2468,2468,2468,2468,2468,2468,0,0,0,0,0,0,0,
  2468,2468,2468,2468,2464,2464,2464,2464,2464,0,0,0,0,0,0,0,
  2464,2464,2464,2460,2460,2460,2460,2460,2460,0,0,0,0,0,0,0,
  2460,2460,2460,2460,2456,2456,2456,2456,2456,0,0,0,0,0,0,0,
  2456,2456,2456,2452,2452,2452,2452,2452,2452,0,0,0,0,0,0,0,
  2452,2452,2452,2452,2448,2448,2448,2448,2448,0,0,0,0,0,0,0,
  2448,2448,2448,2444,2444,2444,2444,2444,2444,0,0,0,0,0,0,0,
  2444,2444,2444,2444,2440,2440,2440,2440,2440,0,0,0,0,0,0,0,
  2440,2440,2440,2436,2436,2436,2436,2436,2436,0,0,0,0,0,0,0,
  2436,2436,2436,2436,2432,2432,2432,2432,2432,0,0,0,0,0,0,0,
  2432,2432,2432,2428,2428,2428,2428,2428,2428,0,0,0,0,0,0,0,
  2428,2428,2428,2428,2424,2424,2424,2424,2424,0,0,0,0,0,0,0,
  2424,2424,2424,2420,2420,2420,2420,2420,2420,0,0,0,0,0,0,0,
  2420,2420,2420,2420,2416,2416,2416,2416,2416,0,0,0,0,0,0,0,
  2416,2416,2416,2412,2412,2412,2412,2412,2412,0,0,0,0,0,0,0,
  2412,2412,2412,2412,2408,2408,2408,2408,2408,0,0,0,0,0,0,0,
  2408,2408,2408,2404,2404,2404,2404,2404,2404,0,0,0,0,0,0,0,
  2404,2404,2404,2404,2400,2400,2400,2400,2400,0,0,0,0,0,0,0,
  2400,2400,2400,2396,2396,2396,2396,2396,2396,0,0,0,0,0,0,0,
  2396,2396,2396,2396,2392,2392,2392,2392,2392,0,0,0,0,0,0,0,
  2392,2392,2392,2388,2388,2388,2388,2388,2388,0,0,0,0,0,0,0,
  2388,2388,2388,2388,2384,2384,2384,2384,2384,0,0,0,0,0,0,0,
  2384,2384,2384,2380,2380,2380,2380,2380,2380,0,0,0,0,0,0,0,
  2380,2380,2380,2380,2376,2376,2376,2376,2376,0,0,0,0,0,0,0,
  2376,2376,2376,2372,2372,2372,2372,2372,2372,0,0,0,0,0,0,0,
  2372,2372,2372,2372,2368,2368,2368,2368,2368,0,0,0,0,0,0,0,
  2368,2368,2368,2364,2364,2364,2364,2364,2364,0,0,0,0,0,0,0,
  2364,2364,2364,2364,2360,2360,2360,2360,2360,0,0,0,0,0,0,0,
  2360,2360,2360,2356,2356,2356,2356,2356,2356,0,0,0,0,0,0,0,
  2356,2356,2356,2356,2352,2352,2352,2352,2352,0,0,0,0,0,0,0,
  2352,2352,2352,2348,2348,2348,2348,2348,2348,0,0,0,0,0,0,0,
  2348,2348,2348,2348,2344,2344,2344,2344,2344,0,0,0,0,0,0,0,
  2344,2344,2344,2340,2340,2340,2340,2340,2340,0,0,0,0,0,0,0,
  2340,2340,2340,2340,2336,2336,2336,2336,2336,0,0,0,0,0,0,0,
  2336,2336,2336,2332,2332,2332,2332,2332,2332,0,0,0,0,0,0,0,
  2332,2332,2332,2332,2328,2328,2328,2328,2328,0,0,0,0,0,0,0,
  2328,2328,2328,2324,2324,2324,2324,2324,2324,0,0,0,0,0,0,0,
  2324,2324,2324,2324,2320,2320,2320,2320,2320,0,0,0,0,0,0,0,
  2320,2320,2320,2316,2316,2316,2316,2316,2316,0,0,0,0,0,0,0,
  2316,2316,2316,2316,2312,2312,2312,2312,2312,0,0,0,0,0,0,0,
  2312,2312,2312,2308,2308,2308,2308,2308,2308,0,0,0,0,0,0,0,
  2308,2308,2308,2308,2304,2304,2304,2304,2304,0,0,0,0,0,0,0,
  2304,2304,2304,2300,2300,2300,2300,2300,2300,0,0,0,0,0,0,0,
  2300,2300,2300,2300,2296,2296,2296,2296,2296,0,0,0,0,0,0,0,
  2296,2296,2296,2292,2292,2292,2292,2292,2292,0,0,0,0,0,0,0,
  2292,2292,2292,2292,2288,2288,2288,2288,2288,0,0,0,0,0,0,0,
  2288,2288,2288,2284,2284,2284,2284,2284,2284,0,0,0,0,0,0,0,
  2284,2284,2284,2284,2280,2280,2280,2280,2280,0,0,0,0,0,0,0,
  2280,2280,2280,2276,2276,2276,2276,2276,2276,0,0,0,0,0,0,0,
  2276,2276,2276,2276,2272,2272,2272,2272,2272,0,0,0,0,0,0,0,
  2272,2272,2272,2268,2268,2268,2268,2268,2268,0,0,0,0,0,0,0,
  2268,2268,2268,2268,2264,2264,2264,2264,2264,0,0,0,0,0,0,0,
  2264,2264,2264,2260,2260,2260,2260,2260,2260,0,0,0,0,0,0,0,
  2260,2260,2260,2260,2256,2256,2256,2256,2256,0,0,0,0,0,0,0,
  2256,2256,2256,2252,2252,2252,2252,2252,2252,0,0,0,0,0,0,0,
  2252,2252,2252,2252,2248,2248,2248,2248,2248,0,0,0,0,0,0,0,
  2248,2248,2248,2244,2244,2244,2244,2244,2244,0,0,0,0,0,0,0,
  2244,2244,2244,2244,2240,2240,2240,2240,2240,0,0,0,0,0,0,0,
  2240,2240,2240,2236,2236,2236,2236,2236,2236,0,0,0,0,0,0,0,
  2236,2236,2236,2236,2232,2232,2232,2232,2232,0,0,0,0,0,0,0,
  2232,2232,2232,2228,2228,2228,2228,2228,2228,0,0,0,0,0,0,0,
  2228,2228,2228,2228,2224,2224,2224,2224,2224,0,0,0,0,0,0,0,
  2224,2224,2224,2220,2220,2220,2220,2220,2220,0,0,0,0,0,0,0,
  2220,2220,2220,2220,2216,2216,2216,2216,2216,0,0,0,0,0,0,0,
  2216,2216,2216,2212,2212,2212,2212,2212,2212,0,0,0,0,0,0,0,
  2212,2212,2212,2212,2208,2208,2208,2208,2208,0,0,0,0,0,0,0,
  2208,2208,2208,2204,2204,2204,2204,2204,2204,0,0,0,0,0,0,0,
  2204,2204,2204,2204,2200,2200,2200,2200,2200,0,0,0,0,0,0,0,
  2200,2200,2200,2196,2196,2196,2196,2196,2196,0,0,0,0,0,0,0,
  2196,2196,2196,2196,2192,2192,2192,2192,2192,0,0,0,0,0,0,0,
  2192,2192,2192,2188,2188,2188,2188,2188,2188,0,0,0,0,0,0,0,
  2188,2188,2188,2188,2184,2184,2184,2184,2184,0,0,0,0,0,0,0,
  2184,2184,2184,2180,2180,2180,2180,2180,2180,0,0,0,0,0,0,0,
  2180,2180,2180,2180,2176,2176,2176,2176,2176,0,0,0,0,0,0,0,
  2176,2176,2176,2172,2172,2172,2172,2172,2172,0,0,0,0,0,0,0,
  2172,2172,2172,2172,2168,2168,2168,2168,2168,0,0,0,0,0,0,0,
  2168,2168,2168,2164,2164,2164,2164,2164,2164,0,0,0,0,0,0,0,
  2164,2164,2164,2164,2160,2160,2160,2160,2160,0,0,0,0,0,0,0,
  2160,2160,2160,2156,2156,2156,2156,2156,2156,0,0,0,0,0,0,0,
  2156,2156,2156,2156,2152,2152,2152,2152,2152,0,0,0,0,0,0,0,
  2152,2152,2152,2148,2148,2148,2148,2148,2148,0,0,0,0,0,0,0,
  2148,2148,2148,2148,2144,2144,2144,2144,2144,0,0,0,0,0,0,0,
  2144,2144,2144,2140,2140,2140,2140,2140,2140,0,0,0,0,0,0,0,
  2140,2140,2140,2140,2136,2136,2136,2136,2136,0,0,0,0,0,0,0,
  2136,2136,2136,2132,2132,2132,2132,2132,2132,0,0,0,0,0,0,0,
  2132,2132,2132,2132,2128,2128,2128,2128,2128,0,0,0,0,0,0,0,
  2128,2128,2128,2124,2124,2124,2124,2124,2124,0,0,0,0,0,0,0,
  2124,2124,2124,2124,2120,2120,2120,2120,2120,0,0,0,0,0,0,0,
  2120,2120,2120,2116,2116,2116,2116,2116,2116,0,0,0,0,0,0,0,
  2116,2116,2116,2116,2112,2112,2112,2112,2112,0,0,0,0,0,0,0,
  2112,2112,2112,2108,2108,2108,2108,2108,2108,0,0,0,0,0,0,0,
  2108,2108,2108,2108,2104,2104,2104,2104,2104,0,0,0,0,0,0,0,
  2104,2104,2104,2100,2100,2100,2100,2100,2100,0,0,0,0,0,0,0,
  2100,2100,2100,2100,2096,2096,2096,2096,2096,0,0,0,0,0,0,0,
  2096,2096,2096,2092,2092,2092,2092,2092,2092,0,0,0,0,0,0,0,
  2092,2092,2092,2092,2088,2088,2088,2088,2088,0,0,0,0,0,0,0,
  2088,2088,2088,2084,2084,2084,2084,2084,2084,0,0,0,0,0,0,0,
  2084,2084,2084,2084,2080,2080,2080,2080,2080,0,0,0,0,0,0,0,
  2080,2080,2080,2076,2076,2076,2076,2076,2076,0,0,0,0,0,0,0,
  2076,2076,2076,2076,2072,2072,2072,2072,2072,0,0,0,0,0,0,0,
  2072,2072,2072,2068,2068,2068,2068,2068,2068,0,0,0,0,0,0,0,
  2068,2068,2068,2068,2064,2064,2064,2064,2064,0,0,0,0,0,0,0,
  2064,2064,2064,2060,2060,2060,2060,2060,2060,0,0,0,0,0,0,0,
  2060,2060,2060,2060,2056,2056,2056,2056,2056,0,0,0,0,0,0,0,
  2056,2056,2056,2052,2052,2052,2052,2052,2052,0,0,0,0,0,0,0,
  2052,2052,2052,2052,2048,2048,2048,2048,2048,0,0,0,0,0,0,0,
  2048,2048,2048,2044,2044,2044,2044,2044,2044,0,0,0,0,0,0,0,
  2044,2044,1916,1916,1916,2040,2040,2040,2040,0,0,0,0,0,0,0,
  2040,2040,1912,1912,1912,2036,2036,2036,2036,0,0,0,0,0,0,0,
  2036,2036,1908,1908,1908,2032,2032,2032,2032,0,0,0,0,0,0,0,
  2032,2032,1904,1904,1904,2028,2028,2028,2028,0,0,0,0,0,0,0,
  2028,2028,1900,1900,1900,2024,2024,2024,2024,0,0,0,0,0,0,0,
  2024,2024,1896,1896,1896,2020,2020,2020,2020,0,0,0,0,0,0,0,
  2020,2020,1892,1892,1892,2016,2016,2016,2016,0,0,0,0,0,0,0,
  2016,2016,1888,1888,1888,2012,2012,2012,2012,0,0,0,0,0,0,0,
  2012,2012,1884,1884,1884,2008,2008,2008,2008,0,0,0,0,0,0,0,
  2008,2008,1880,1880,1880,2004,2004,2004,2004,0,0,0,0,0,0,0,
  2004,2004,1876,1876,1876,2000,2000,2000,2000,0,0,0,0,0,0,0,
  2000,2000,1872,1872,1872,1996,1996,1996,1996,0,0,0,0,0,0,0,
  1996,1996,1868,1868,1868,1992,1992,1992,1992,0,0,0,0,0,0,0,
  1992,1992,1864,1864,1864,1988,1988,1988,1988,0,0,0,0,0,0,0,
  1988,1988,1860,1860,1860,1984,1984,1984,1984,0,0,0,0,0,0,0,
  1984,1984,1856,1856,1856,1980,1980,1980,1980,0,0,0,0,0,0,0,
  1980,1980,1852,1852,1852,1976,1976,1976,1976,0,0,0,0,0,0,0,
  1976,1976,1848,1848,1848,1972,1972,1972,1972,0,0,0,0,0,0,0,
  1972,1972,1844,1844,1844,1968,1968,1968,1968,0,0,0,0,0,0,0,
  1968,1968,1840,1840,1840,1964,1964,1964,1964,0,0,0,0,0,0,0,
  1964,1964,1836,1836,1836,1960,1960,1960,1960,0,0,0,0,0,0,0,
  1960,1960,1832,1832,1832,1956,1956,1956,1956,0,0,0,0,0,0,0,
  1956,1956,1828,1828,1828,1952,1952,1952,1952,0,0,0,0,0,0,0,
  1952,1952,1824,1824,1824,1948,1948,1948,1948,0,0,0,0,0,0,0,
  1948,1948,1820,1820,1820,1944,1944,1944,1944,0,0,0,0,0,0,0,
  1944,1944,1816,1816,1816,1940,1940,1940,1940,0,0,0,0,0,0,0,
  1940,1940,1812,1812,1812,1936,1936,1936,1936,0,0,0,0,0,0,0,
  1936,1936,1808,1808,1808,1932,1932,1932,1932,0,0,0,0,0,0,0,
  1932,1932,1804,1804,1804,1928,1928,1928,1928,0,0,0,0,0,0,0,
  1928,1928,1800,1800,1800,1924,1924,1924,1924,0,0,0,0,0,0,0,
  1924,1924,1796,1796,1796,1920,1920,1920,1920,0,0,0,0,0,0,0,
  1920,1920,1792,1792,1792,1788,1788,1788,1788,0,0,0,0,0,0,0,
  1788,1788,1660,1660,1660,1784,1784,1784,1784,0,0,0,0,0,0,0,
  1784,1784,1656,1656,1656,1780,1780,1780,1780,0,0,0,0,0,0,0,
  1780,1780,1652,1652,1652,1776,1776,1776,1776,0,0,0,0,0,0,0,
  1776,1776,1648,1648,1648,1772,1772,1772,1772,0,0,0,0,0,0,0,
  1772,1772,1644,1644,1644,1768,1768,1768,1768,0,0,0,0,0,0,0,
  1768,1768,1640,1640,1640,1764,1764,1764,1764,0,0,0,0,0,0,0,
  1764,1764,1636,1636,1636,1760,1760,1760,1760,0,0,0,0,0,0,0,
  1760,1760,1632,1632,1632,1756,1756,1756,1756,0,0,0,0,0,0,0,
  1756,1756,1628,1628,1628,1752,1752,1752,1752,0,0,0,0,0,0,0,
  1752,1752,1624,1624,1624,1748,1748,1748,1748,0,0,0,0,0,0,0,
  1748,1748,1620,1620,1620,1744,1744,1744,1744,0,0,0,0,0,0,0,
  1744,1744,1616,1616,1616,1740,1740,1740,1740,0,0,0,0,0,0,0,
  1740,1740,1612,1612,1612,1736,1736,1736,1736,0,0,0,0,0,0,0,
  1736,1736,1608,1608,1608,1732,1732,1732,1732,0,0,0,0,0,0,0,
  1732,1732,1604,1604,1604,1728,1728,1728,1728,0,0,0,0,0,0,0,
  1728,1728,1600,1600,1600,1724,1724,1724,1724,0,0,0,0,0,0,0,
  1724,1724,1596,1596,1596,1720,1720,1720,1720,0,0,0,0,0,0,0,
  1720,1720,1592,1592,1592,1716,1716,1716,1716,0,0,0,0,0,0,0,
  1716,1716,1588,1588,1588,1712,1712,1712,1712,0,0,0,0,0,0,0,
  1712,1712,1584,1584,1584,1708,1708,1708,1708,0,0,0,0,0,0,0,
  1708,1708,1580,1580,1580,1704,1704,1704,1704,0,0,0,0,0,0,0,
  1704,1704,1576,1576,1576,1700,1700,1700,1700,0,0,0,0,0,0,0,
  1700,1700,1572,1572,1572,1696,1696,1696,1696,0,0,0,0,0,0,0,
  1696,1696,1568,1568,1568,1692,1692,1692,1692,0,0,0,0,0,0,0,
  1692,1692,1564,1564,1564,1688,1688,1688,1688,0,0,0,0,0,0,0,
  1688,1688,1560,1560,1560,1684,1684,1684,1684,0,0,0,0,0,0,0,
  1684,1684,1556,1556,1556,1680,1680,1680,1680,0,0,0,0,0,0,0,
  1680,1680,1552,1552,1552,1676,1676,1676,1676,0,0,0,0,0,0,0,
  1676,1676,1548,1548,1548,1672,1672,1672,1672,0,0,0,0,0,0,0,
  1672,1672,1544,1544,1544,1668,1668,1668,1668,0,0,0,0,0,0,0,
  1668,1668,1540,1540,1540,1664,1664,1664,1664,0,0,0,0,0,0,0,
  1664,1664,1536,1536,1536,1532,1532,1532,1532,0,0,0,0,0,0,0,
  1532,1404,1276,1276,1276,1148,1528,1528,1528,0,0,0,0,0,0,0,
  1528,1400,1272,1272,1272,1144,1524,1524,1524,0,0,0,0,0,0,0,
  1524,1396,1268,1268,1268,1140,1520,1520,1520,0,0,0,0,0,0,0,
  1520,1392,1264,1264,1264,1136,1516,1516,1516,0,0,0,0,0,0,0,
  1516,1388,1260,1260,1260,1132,1512,1512,1512,0,0,0,0,0,0,0,
  1512,1384,1256,1256,1256,1128,1508,1508,1508,0,0,0,0,0,0,0,
  1508,1380,1252,1252,1252,1124,1504,1504,1504,0,0,0,0,0,0,0,
  1504,1376,1248,1248,1248,1120,1500,1500,1500,0,0,0,0,0,0,0,
  1500,1372,1244,1244,1244,1116,1496,1496,1496,0,0,0,0,0,0,0,
  1496,1368,1240,1240,1240,1112,1492,1492,1492,0,0,0,0,0,0,0,
  1492,1364,1236,1236,1236,1108,1488,1488,1488,0,0,0,0,0,0,0,
  1488,1360,1232,1232,1232,1104,1484,1484,1484,0,0,0,0,0,0,0,
  1484,1356,1228,1228,1228,1100,1480,1480,1480,0,0,0,0,0,0,0,
  1480,1352,1224,1224,1224,1096,1476,1476,1476,0,0,0,0,0,0,0,
  1476,1348,1220,1220,1220,1092,1472,1472,1472,0,0,0,0,0,0,0,
  1472,1344,1216,1216,1216,1088,1468,1468,1468,0,0,0,0,0,0,0,
  1468,1340,1212,1212,1212,1084,1464,1464,1464,0,0,0,0,0,0,0,
  1464,1336,1208,1208,1208,1080,1460,1460,1460,0,0,0,0,0,0,0,
  1460,1332,1204,1204,1204,1076,1456,1456,1456,0,0,0,0,0,0,0,
  1456,1328,1200,1200,1200,1072,1452,1452,1452,0,0,0,0,0,0,0,
  1452,1324,1196,1196,1196,1068,1448,1448,1448,0,0,0,0,0,0,0,
  1448,1320,1192,1192,1192,1064,1444,1444,1444,0,0,0,0,0,0,0,
  1444,1316,1188,1188,1188,1060,1440,1440,1440,0,0,0,0,0,0,0,
  1440,1312,1184,1184,1184,1056,1436,1436,1436,0,0,0,0,0,0,0,
  1436,1308,1180,1180,1180,1052,1432,1432,1432,0,0,0,0,0,0,0,
  1432,1304,1176,1176,1176,1048,1428,1428,1428,0,0,0,0,0,0,0,
  1428,1300,1172,1172,1172,1044,1424,1424,1424,0,0,0,0,0,0,0,
  1424,1296,1168,1168,1168,1040,1420,1420,1420,0,0,0,0,0,0,0,
  1420,1292,1164,1164,1164,1036,1416,1416,1416,0,0,0,0,0,0,0,
  1416,1288,1160,1160,1160,1032,1412,1412,1412,0,0,0,0,0,0,0,
  1412,1284,1156,1156,1156,1028,1408,1408,1408,0,0,0,0,0,0,0,
  1408,1280,1152,1152,1152,1024,1020,1020,892,0,0,0,0,0,0,0,
  892,764,636,508,380,252,124,1016,888,0,0,0,0,0,0,0,
  888,760,632,504,376,248,120,1012,884,0,0,0,0,0,0,0,
  884,756,628,500,372,244,116,1008,880,0,0,0,0,0,0,0,
  880,752,624,496,368,240,112,1004,876,0,0,0,0,0,0,0,
  876,748,620,492,364,236,108,1000,872,0,0,0,0,0,0,0,
  872,744,616,488,360,232,104,996,868,0,0,0,0,0,0,0,
  868,740,612,484,356,228,100,992,864,0,0,0,0,0,0,0,
  864,736,608,480,352,224,96,988,860,0,0,0,0,0,0,0,
  860,732,604,476,348,220,92,984,856,0,0,0,0,0,0,0,
  856,728,600,472,344,216,88,980,852,0,0,0,0,0,0,0,
  852,724,596,468,340,212,84,976,848,0,0,0,0,0,0,0,
  848,720,592,464,336,208,80,972,844,0,0,0,0,0,0,0,
  844,716,588,460,332,204,76,968,840,0,0,0,0,0,0,0,
  840,712,584,456,328,200,72,964,836,0,0,0,0,0,0,0,
  836,708,580,452,324,196,68,960,832,0,0,0,0,0,0,0,
  832,704,576,448,320,192,64,956,828,0,0,0,0,0,0,0,
  828,700,572,444,316,188,60,952,824,0,0,0,0,0,0,0,
  824,696,568,440,312,184,56,948,820,0,0,0,0,0,0,0,
  820,692,564,436,308,180,52,944,816,0,0,0,0,0,0,0,
  816,688,560,432,304,176,48,940,812,0,0,0,0,0,0,0,
  812,684,556,428,300,172,44,936,808,0,0,0,0,0,0,0,
  808,680,552,424,296,168,40,932,804,0,0,0,0,0,0,0,
  804,676,548,420,292,164,36,928,800,0,0,0,0,0,0,0,
  800,672,544,416,288,160,32,924,796,0,0,0,0,0,0,0,
  796,668,540,412,284,156,28,920,792,0,0,0,0,0,0,0,
  792,664,536,408,280,152,24,916,788,0,0,0,0,0,0,0,
  788,660,532,404,276,148,20,912,784,0,0,0,0,0,0,0,
  784,656,528,400,272,144,16,908,780,0,0,0,0,0,0,0,
  780,652,524,396,268,140,12,904,776,0,0,0,0,0,0,0,
  776,648,520,392,264,136,8,900,772,0,0,0,0,0,0,0,
  772,644,516,388,260,132,4,896,768,0,0,0,0,0,0,0,
  768,640,512,384,256,128,0,2560,2688,0,0,0,0,0,0,0,
};
constexpr int UT_ROW = 4608;
constexpr int KI_ARR = 4160;
constexpr int KI_B = 2 * KI_ARR;
constexpr int KG_ROW = 272;
constexpr int KG_LDS = 53 * KG_ROW;

typedef bf16x8 bf16x8_a4 __attribute__((aligned(4)));
struct cpx { float r, i; };
__device__ __forceinline__ cpx cmul(cpx a, cpx b) { return {a.r * b.r - a.i * b.i, a.r * b.i + a.i * b.r}; }
__device__ __forceinline__ cpx cexpm(float ar2, float rev, float m) {
    const float e = __builtin_amdgcn_exp2f(ar2 * m);
    const float p = __builtin_amdgcn_fractf(rev * m);
    return {e * __builtin_amdgcn_cosf(p), e * __builtin_amdgcn_sinf(p)};
}
__device__ __forceinline__ unsigned pkneg(float a, float b) { unsigned r; asm("v_cvt_pk_bf16_f32 %0, %1, -%2" : "=v"(r) : "v"(a), "v"(b)); return r; }
__device__ __forceinline__ float rbf16(float f) { return __uint_as_float(pk(f, 0.f) << 16); }

struct KgPar { float a, rv; cpx W; };
__device__ __forceinline__ KgPar kgen_params(const u16* __restrict__ Are, const u16* __restrict__ Aim, const u16* __restrict__ Bre, const u16* __restrict__ Bim,
        const u16* __restrict__ Cre, const u16* __restrict__ Cim, const u16* __restrict__ logdt, int d, int hf, int lane) {
    const int o = d * NS + hf * 64 + lane;
    const float dt = rbf16(__builtin_amdgcn_exp2f(bf2f(logdt[d]) * 1.4426950408889634f));
    const float ar = dt * bf2f(Are[o]), ai = dt * bf2f(Aim[o]);
    const cpx Bc = {dt * bf2f(Bre[o]), dt * bf2f(Bim[o])}, Cc = {bf2f(Cre[o]), bf2f(Cim[o])};
    return {ar * 1.4426950408889634f, ai * 0.15915494309189535f, cmul(Cc, Bc)};
}
__device__ __forceinline__ void kgen_Y(const KgPar& P, LDS char* lw, int lane) {
    LDS unsigned* yw = (LDS unsigned*)(lw + 21 * KG_ROW) + lane;
    #define PY(row, v) yw[(row) * (KG_ROW / 4)] = pk((v).r, (v).i)
    const cpx w8 = cexpm(P.a, P.rv, 8.f), f64 = cexpm(P.a, P.rv, 64.f);
    cpx p[8]; p[0] = {1.f, 0.f}; p[1] = w8; p[2] = cmul(w8, w8); p[3] = cmul(p[2], w8); p[4] = cmul(p[2], p[2]); p[5] = cmul(p[4], w8); p[6] = cmul(p[4], p[2]); p[7] = cmul(p[4], p[3]);
    #pragma unroll
    for (int b = 0; b < 8; b++) PY(b, p[b]);
    const cpx f128 = cmul(f64, f64), f192 = cmul(f128, f64);
    PY(8, f64); PY(16, f128); PY(24, f192);
    #pragma unroll
    for (int b = 1; b < 8; b++) { cpx v = cmul(f64, p[b]); PY(8 + b, v); v = cmul(f128, p[b]); PY(16 + b, v); v = cmul(f192, p[b]); PY(24 + b, v); }
    #undef PY
}
__device__ __forceinline__ void kgen_X(const KgPar& P, LDS char* lw, int lane) {
    LDS unsigned* xw = (LDS unsigned*)(lw) + lane;
    #define PX(row, v) xw[(row) * (KG_ROW / 4)] = pkneg((v).r, (v).i)
    const cpx Wn = P.W;
    const cpx w1 = cexpm(P.a, P.rv, 1.f), w8 = cexpm(P.a, P.rv, 8.f), f256 = cexpm(P.a, P.rv, 256.f), f1024 = cexpm(P.a, P.rv, 1024.f);
    const cpx w2 = cmul(w1, w1), w3 = cmul(w2, w1), w4 = cmul(w2, w2), w6 = cmul(w4, w2);
    const cpx x8 = cmul(Wn, w8), x4 = cmul(Wn, w4);
    { cpx v; v = cmul(Wn, w1); PX(0, v); v = cmul(Wn, w2); PX(1, v); v = cmul(Wn, w3); PX(2, v); PX(3, x4); PX(7, x8); PX(20, Wn);
      v = cmul(x4, w1); PX(4, v); v = cmul(x4, w2); PX(5, v); v = cmul(x4, w3); PX(6, v); }
    const cpx g256 = cmul(Wn, f256), f512 = cmul(f256, f256), f768 = cmul(f512, f256);
    { cpx v; v = cmul(g256, w2); PX(8, v); v = cmul(g256, w4); PX(9, v); v = cmul(g256, w6); PX(10, v); v = cmul(g256, w8); PX(11, v); }
    { const cpx g512 = cmul(Wn, f512), g768 = cmul(Wn, f768); cpx v;
      v = cmul(g512, w4); PX(12, v); v = cmul(g512, w8); PX(13, v); v = cmul(g768, w4); PX(14, v); v = cmul(g768, w8); PX(15, v); }
    { const cpx y = cmul(x8, f1024); PX(16, y); cpx v = cmul(y, f256); PX(17, v); v = cmul(y, f512); PX(18, v); v = cmul(y, f768); PX(19, v); }
    #undef PX
}
__device__ __forceinline__ void kgen_contract(LDS char* lw, int lane) {
    const int r32 = lane & 31, h = lane >> 5;
    f32x16 acc;
    #pragma unroll
    for (int i = 0; i < 16; i++) acc[i] = 0;
    const int xr = r32 < 21 ? r32 : 20;
    LDS char* xa = lw + xr * KG_ROW + h * 16;
    LDS char* yb = lw + (21 + r32) * KG_ROW + h * 16;
    bf16x8 fa[8], fb[8];
    #pragma unroll
    for (int ks = 0; ks < 8; ks++) { fa[ks] = *(LDS bf16x8*)(xa + ks * 32); fb[ks] = *(LDS bf16x8*)(yb + ks * 32); }
    #pragma unroll
    for (int ks = 0; ks < 8; ks++) acc = MFMA32(fa[ks], fb[ks], acc);
    LDS float* T = (LDS float*)lw;
    #pragma unroll
    for (int i = 0; i < 12; i++) { const int row = (i & 3) + 8 * (i >> 2) + 4 * h; if (row <= 20) T[row * 32 + r32] = acc[i]; }
    if (lane == 0) T[21 * 32] = 0.f;
}
template<class P4>
__device__ __forceinline__ void kgen_expand_to(P4* dst, LDS char* t0, u32x4 tb, unsigned tb8, int q, int lane) {
    float v[9];
    #pragma unroll
    for (int t = 0; t < 9; t++) {
        const unsigned of = t == 8 ? tb8 : ((t & 1) ? (tb[t >> 1] >> 16) : (tb[t >> 1] & 0xffffu));
        LDS char* p = t0 + of;
        v[t] = *(LDS float*)p + *(LDS float*)(p + KG_LDS);
    }
    dst[q * 64 + lane] = (u32x4){pk(v[0], v[1]), pk(v[2], v[3]), pk(v[4], v[5]), pk(v[6], v[7])};
    dst[KI_ARR / 16 + q * 64 + lane] = (u32x4){pk(v[1], v[2]), pk(v[3], v[4]), pk(v[5], v[6]), pk(v[7], v[8])};
    if (q == 0 && lane < 8) dst[(lane >> 2) * (KI_ARR / 16) + 256 + (lane & 3)] = (u32x4)0u;
}
__device__ __forceinline__ void kgen_expand(LDS char* dst, LDS char* t0, u32x4 tb, unsigned tb8, int q, int lane) { kgen_expand_to((LDS u32x4*)dst, t0, tb, tb8, q, lane); }

__device__ __forceinline__ void conv_units2(f32x4 (&acc)[8], LDS char* lk, LDS char* lu, int e0_first, int lane) {
    const int b = lane & 15, g = lane >> 4;
    LDS char* ke = lk + ((b & 1) ? 0 : KI_ARR) + 4 * (((2031 - b) >> 1) + 4 * g) - 64 * e0_first;
    LDS char* ue = lu + 480 + 32 * b + 16 * g - 64 * e0_first;
    bf16x8 A[2][8], B[2][8];
    #pragma unroll
    for (int z = 0; z < 2; z++)
        #pragma unroll
        for (int i = 0; i < 8; i++) { A[z][i] = *(LDS bf16x8_a4*)(ke - 64 * z - 512 * i); B[z][i] = *(LDS bf16x8*)(ue - 64 * z + 512 * i); }
    #pragma unroll
    for (int z = 0; z < 2; z++)
        #pragma unroll
        for (int c = 0; c < 8; c++)
            #pragma unroll
            for (int e1 = 0; e1 <= c; e1++) acc[c] = MFMA16(A[z][e1], B[z][c - e1], acc[c]);
}

template<int I>
__device__ __forceinline__ void conv_shared_case(f32x4 (&acc)[2], LDS char* ka, LDS char* ub, int e0_first) {
    #pragma unroll
    for (int z = 0; z < 2; z++) {
        const int e0 = e0_first + z;
        bf16x8 A[8 - I], B[8 - I];
        LDS char* ke = ka - 64 * e0; LDS char* ue = ub - 64 * e0;
        #pragma unroll
        for (int i = 0; i < 8 - I; i++) { A[i] = *(LDS bf16x8_a4*)(ke - 512 * i); B[i] = *(LDS bf16x8*)(ue + 512 * i); }
        #pragma unroll
        for (int e1 = 0; e1 <= I; e1++) acc[0] = MFMA16(A[e1], B[I - e1], acc[0]);
        #pragma unroll
        for (int e1 = 0; e1 <= 7 - I; e1++) acc[1] = MFMA16(A[e1], B[7 - I - e1], acc[1]);
    }
}
__device__ __forceinline__ void conv_shared(f32x4 (&acc)[2], LDS char* lk, LDS char* lu, int e0_first, int iq, int lane) {
    const int b = lane & 15, g = lane >> 4;
    LDS char* ka = lk + ((b & 1) ? 0 : KI_ARR) + 4 * (((2031 - b) >> 1) + 4 * g);
    LDS char* ub = lu + 480 + 32 * b + 16 * g;
    if (iq == 0) conv_shared_case<0>(acc, ka, ub, e0_first);
    else if (iq == 1) conv_shared_case<1>(acc, ka, ub, e0_first);
    else if (iq == 2) conv_shared_case<2>(acc, ka, ub, e0_first);
    else conv_shared_case<3>(acc, ka, ub, e0_first);
}
constexpr int K2_CODE_BYTES = 26624, K2_SELF_BYTES = 26112;
constexpr int NFLAGS = 3072;
struct UtTile { u32x4 v[8]; };
__device__ __forceinline__ UtTile ut_load(const u16* __restrict__ u, int db, int tb, int lane) {
    UtTile t;
    #pragma unroll
    for (int i = 0; i < 8; i++) t.v[i] = *(const u32x4*)((const char*)u + (size_t)(tb * 64 + i * 8 + (lane >> 3)) * 1152 + db * 128 + (lane & 7) * 16);
    return t;
}
__device__ __forceinline__ void ut_finish(const UtTile& t, char* __restrict__ ut, LDS char* lw, int db, int tb, int lane) {
    constexpr int RS = 144;
    #pragma unroll
    for (int i = 0; i < 8; i++) *(LDS u32x4*)(lw + (i * 8 + (lane >> 3)) * RS + (lane & 7) * 16) = t.v[i];
    const int g = lane >> 4, q4 = (lane & 15) >> 2, p4 = lane & 3;
    #pragma unroll
    for (int c = 0; c < 4; c++)
        #pragma unroll
        for (int hf = 0; hf < 2; hf++) {
            const int t0 = hf * 32 + g * 8;
            bf16x4 a = tr4(lw + (t0 + q4) * RS + c * 32 + p4 * 8), b = tr4(lw + (t0 + 4 + q4) * RS + c * 32 + p4 * 8);
            const int d = db * 64 + c * 16 + (lane & 15);
            *(bf16x8*)(ut + (size_t)d * UT_ROW + 512 + (tb * 64 + t0) * 2) = cat8(a, b);
        }
}
constexpr int K1_LDS = 4 * KG_LDS + 8 * 9216;
struct K1Args { const u16* x; const u16* wqkv; const u16* u; const u16* Are; const u16* Aim; const u16* Bre; const u16* Bim; const u16* Cre; const u16* Cim; const u16* logdt;
                char* vimg; char* ut; char* kimg; unsigned* flags; const u16* wo; };
__global__ __launch_bounds__(512, 2) void k1_kernel(K1Args a) {
    extern __shared__ __attribute__((aligned(16))) char smem[];
    const int b = blockIdx.x, lane = threadIdx.x & 63, w = __builtin_amdgcn_readfirstlane(threadIdx.x >> 6);
    if (b >= 224 && b < 232 && w == 7) {
        unsigned off = lane * 16; int n = K2_CODE_BYTES >> 10;
        asm volatile("s_getpc_b64 s[40:41]\n\ts_add_u32 s40, s40, _Z9k2_kernel6K2Args@rel32@lo+4\n\ts_addc_u32 s41, s41, _Z9k2_kernel6K2Args@rel32@hi+12\n"
                     "1:\n\tglobal_load_dwordx4 v[200:203], %0, s[40:41]\n\tv_add_u32 %0, 1024, %0\n\ts_sub_u32 %1, %1, 1\n\ts_cmp_lg_u32 %1, 0\n\ts_cbranch_scc1 1b"
                     : "+v"(off), "+s"(n) :: "memory", "scc", "s40", "s41", "v200", "v201", "v202", "v203");
    }
    if (b >= 224) {
        if (b == 255) for (int i = threadIdx.x; i < NFLAGS; i += 512) a.flags[i] = 0u;
        LDS char* sm = (LDS char*)smem;
        const int area = w >> 1, sidx = 2 * (b - 224) + (area >> 1);
        KgPar kp = kgen_params(a.Are, a.Aim, a.Bre, a.Bim, a.Cre, a.Cim, a.logdt, 72 * (sidx >> 3) + 64 + (sidx & 7), area & 1, lane);
        const u32x4 tb = *(const u32x4*)(KEXP_TAB + ((w & 3) * 64 + lane) * 16);
        const unsigned tb8 = KEXP_TAB[((w & 3) * 64 + lane) * 16 + 8];
        if (w == 2) {
            #pragma unroll
            for (int i = 0; i < 9; i++) *(u32x4*)(a.ut + (size_t)((b - 224) * 18 + i * 2 + (lane >> 5)) * UT_ROW + (lane & 31) * 16) = (u32x4)0u;
        }
        const int id0 = (b - 224) * 9 + w, id1 = (b - 224) * 9 + 8;
        const UtTile t0 = ut_load(a.u, id0 % 9, id0 / 9, lane);
        UtTile t1;
        if (w == 1) t1 = ut_load(a.u, id1 % 9, id1 / 9, lane);
        LDS char* tbuf = sm + 4 * KG_LDS + w * 9216;
        if ((w & 1) == 0) kgen_Y(kp, sm + area * KG_LDS, lane); else kgen_X(kp, sm + area * KG_LDS, lane);
        ut_finish(t0, a.ut, tbuf, id0 % 9, id0 / 9, lane);
        asm volatile("s_waitcnt lgkmcnt(0)\n\ts_barrier" ::: "memory");
        if ((w & 1) == 0) kgen_contract(sm + area * KG_LDS, lane);
        else if (w == 1) ut_finish(t1, a.ut, tbuf, id1 % 9, id1 / 9, lane);
        asm volatile("s_waitcnt lgkmcnt(0)\n\ts_barrier" ::: "memory");
        kgen_expand_to((u32x4*)(a.kimg + (size_t)(2 * (b - 224) + (w >> 2)) * KI_B), sm + 2 * (w >> 2) * KG_LDS, tb, tb8, w & 3, lane);
        return;
    }
    const int xcd = b & 7, idx = b >> 3;
    const int cb = (xcd & 1) * 7 + idx % 7, tt = (xcd >> 1) * 4 + idx / 7;
    if (w < 4) {
        if (b < 216) {
            const u16* p = b < 36 ? a.Are : b < 72 ? a.Aim : b < 108 ? a.Bre : b < 144 ? a.Bim : b < 180 ? a.Cre : a.Cim;
            const char* pb = (const char*)p + (b % 36) * 4096 + w * 1024;
            asm volatile("global_load_dwordx4 v[200:203], %0, %1" :: "v"(lane * 16), "s"(pb) : "memory", "v200", "v201", "v202", "v203");
        }
        if (b < 216 && w < 3) {
            const char* pb = (const char*)a.wo + (b * 3 + w) * 1024;
            asm volatile("global_load_dwordx4 v[200:203], %0, %1" :: "v"(lane * 16), "s"(pb) : "memory", "v200", "v201", "v202", "v203");
        }
    }
    if (w >= 4) qkv_load(a.x, a.wqkv, a.vimg, smem, cb, tt, w - 4, lane);
    else if (cb >= 9) qkv_consume<true>(a.vimg, smem, cb, tt, w, lane);
    else qkv_consume<false>(a.vimg, smem, cb, tt, w, lane);
}
#define ATT8_A \
    "ds_read_b64 v[80:81], v99\n\t" \
    "ds_read_b128 v[76:79], v98\n\t" \
    "ds_read_b128 v[82:85], v100\n\t" \
    "ds_read_b128 v[86:89], v100 offset:608\n\t" \
    "s_waitcnt lgkmcnt(0)\n\t" \
    "v_mfma_f32_32x32x8_bf16 v[32:47], v[80:81], v[68:69], 0\n\t" \
    "v_mfma_f32_32x32x16_bf16 v[32:47], v[76:79], v[64:67], v[32:47]\n\t" \
    "s_nop 15\n\t" \
    "v_mfma_f32_32x32x8_bf16 v[48:63], v[80:81], v[74:75], 0\n\t" \
    "ds_read_b64 v[110:111], v99 offset:2496\n\t" \
    "ds_read_b128 v[106:109], v98 offset:2496\n\t" \
    "v_exp_f32 v32, v32\n\t" \
    "v_exp_f32 v33, v33\n\t" \
    "v_exp_f32 v34, v34\n\t" \
    "v_exp_f32 v35, v35\n\t" \
    "v_cvt_pk_bf16_f32 v90, v32, v33\n\t" \
    "v_cvt_pk_bf16_f32 v91, v34, v35\n\t" \
    "ds_read_b128 v[112:115], v100 offset:2496\n\t" \
    "v_exp_f32 v36, v36\n\t" \
    "v_exp_f32 v37, v37\n\t" \
    "v_exp_f32 v38, v38\n\t" \
    "v_exp_f32 v39, v39\n\t" \
    "v_cvt_pk_bf16_f32 v92, v36, v37\n\t" \
    "v_cvt_pk_bf16_f32 v93, v38, v39\n\t" \
    "v_mfma_f32_32x32x16_bf16 v[48:63], v[76:79], v[70:73], v[48:63]\n\t" \
    "v_exp_f32 v40, v40\n\t" \
    "v_exp_f32 v41, v41\n\t" \
    "v_exp_f32 v42, v42\n\t" \
    "v_mfma_f32_32x32x16_bf16 v[0:15], v[82:85], v[90:93], 0\n\t" \
    "v_exp_f32 v43, v43\n\t" \
    "v_cvt_pk_bf16_f32 v94, v40, v41\n\t" \
    "v_cvt_pk_bf16_f32 v95, v42, v43\n\t" \
    "v_exp_f32 v44, v44\n\t" \
    "v_exp_f32 v45, v45\n\t" \
    "v_exp_f32 v46, v46\n\t" \
    "v_exp_f32 v47, v47\n\t" \
    "v_cvt_pk_bf16_f32 v96, v44, v45\n\t" \
    "v_cvt_pk_bf16_f32 v97, v46, v47\n\t" \
    "s_waitcnt lgkmcnt(0)\n\t" \
    "v_mfma_f32_32x32x8_bf16 v[32:47], v[110:111], v[68:69], 0\n\t" \
    "v_exp_f32 v48, v48\n\t" \
    "v_exp_f32 v49, v49\n\t" \
    "v_exp_f32 v50, v50\n\t" \
    "v_exp_f32 v51, v51\n\t" \
    "v_cvt_pk_bf16_f32 v90, v48, v49\n\t" \
    "v_cvt_pk_bf16_f32 v91, v50, v51\n\t" \
    "v_mfma_f32_32x32x16_bf16 v[0:15], v[86:89], v[94:97], v[0:15]\n\t" \
    "v_exp_f32 v52, v52\n\t" \
    "v_exp_f32 v53, v53\n\t" \
    "v_exp_f32 v54, v54\n\t" \
    "v_exp_f32 v55, v55\n\t" \
    "v_cvt_pk_bf16_f32 v92, v52, v53\n\t" \
    "v_cvt_pk_bf16_f32 v93, v54, v55\n\t" \
    "v_mfma_f32_32x32x16_bf16 v[32:47], v[106:109], v[64:67], v[32:47]\n\t" \
    "s_add_u32 m0, %[so], %[ldsb]\n\t" \
    "s_nop 0\n\t" \
    "buffer_load_dwordx4 v101, %[srd], %[so] offen lds\n\t" \
    "s_add_u32 %[so], %[so], 4096\n\t" \
    "v_exp_f32 v56, v56\n\t" \
    "v_exp_f32 v57, v57\n\t" \
    "v_exp_f32 v58, v58\n\t" \
    "v_mfma_f32_32x32x16_bf16 v[16:31], v[82:85], v[90:93], 0\n\t" \
    "v_exp_f32 v59, v59\n\t" \
    "v_cvt_pk_bf16_f32 v94, v56, v57\n\t" \
    "v_cvt_pk_bf16_f32 v95, v58, v59\n\t" \
    "v_exp_f32 v60, v60\n\t" \
    "v_exp_f32 v61, v61\n\t" \
    "v_exp_f32 v62, v62\n\t" \
    "v_exp_f32 v63, v63\n\t" \
    "v_cvt_pk_bf16_f32 v96, v60, v61\n\t" \
    "v_cvt_pk_bf16_f32 v97, v62, v63\n\t" \
    "v_mfma_f32_32x32x8_bf16 v[48:63], v[110:111], v[74:75], 0\n\t" \
    "ds_read_b64 v[80:81], v99 offset:4992\n\t" \
    "ds_read_b128 v[76:79], v98 offset:4992\n\t" \
    "v_exp_f32 v32, v32\n\t" \
    "v_exp_f32 v33, v33\n\t" \
    "v_exp_f32 v34, v34\n\t" \
    "v_exp_f32 v35, v35\n\t" \
    "v_cvt_pk_bf16_f32 v90, v32, v33\n\t" \
    "v_cvt_pk_bf16_f32 v91, v34, v35\n\t" \
    "v_mfma_f32_32x32x16_bf16 v[16:31], v[86:89], v[94:97], v[16:31]\n\t" \
    "ds_read_b128 v[86:89], v100 offset:3104\n\t" \
    "ds_read_b128 v[82:85], v100 offset:4992\n\t" \
    "v_exp_f32 v36, v36\n\t" \
    "v_exp_f32 v37, v37\n\t" \
    "v_exp_f32 v38, v38\n\t" \
    "v_exp_f32 v39, v39\n\t" \
    "v_cvt_pk_bf16_f32 v92, v36, v37\n\t" \
    "v_cvt_pk_bf16_f32 v93, v38, v39\n\t" \
    "v_mfma_f32_32x32x16_bf16 v[48:63], v[106:109], v[70:73], v[48:63]\n\t" \
    "v_exp_f32 v40, v40\n\t" \
    "v_exp_f32 v41, v41\n\t" \
    "v_exp_f32 v42, v42\n\t" \
    "v_mfma_f32_32x32x16_bf16 v[0:15], v[112:115], v[90:93], v[0:15]\n\t" \
    "v_exp_f32 v43, v43\n\t" \
    "v_cvt_pk_bf16_f32 v94, v40, v41\n\t" \
    "v_cvt_pk_bf16_f32 v95, v42, v43\n\t" \
    "v_exp_f32 v44, v44\n\t" \
    "v_exp_f32 v45, v45\n\t" \
    "v_exp_f32 v46, v46\n\t" \
    "v_exp_f32 v47, v47\n\t" \
    "v_cvt_pk_bf16_f32 v96, v44, v45\n\t" \
    "v_cvt_pk_bf16_f32 v97, v46, v47\n\t" \
    "s_waitcnt lgkmcnt(0)\n\t" \
    "v_mfma_f32_32x32x8_bf16 v[32:47], v[80:81], v[68:69], 0\n\t" \
    "v_exp_f32 v48, v48\n\t" \
    "v_exp_f32 v49, v49\n\t" \
    "v_exp_f32 v50, v50\n\t" \
    "v_exp_f32 v51, v51\n\t" \
    "v_cvt_pk_bf16_f32 v90, v48, v49\n\t" \
    "v_cvt_pk_bf16_f32 v91, v50, v51\n\t" \
    "v_mfma_f32_32x32x16_bf16 v[0:15], v[86:89], v[94:97], v[0:15]\n\t" \
    "v_exp_f32 v52, v52\n\t" \
    "v_exp_f32 v53, v53\n\t" \
    "v_exp_f32 v54, v54\n\t" \
    "v_exp_f32 v55, v55\n\t" \
    "v_cvt_pk_bf16_f32 v92, v52, v53\n\t" \
    "v_cvt_pk_bf16_f32 v93, v54, v55\n\t" \
    "v_mfma_f32_32x32x16_bf16 v[32:47], v[76:79], v[64:67], v[32:47]\n\t" \
    "s_add_u32 m0, %[so], %[ldsb]\n\t" \
    "s_nop 0\n\t" \
    "buffer_load_dwordx4 v101, %[srd], %[so] offen lds\n\t" \
    "s_add_u32 %[so], %[so], 4096\n\t" \
    "v_exp_f32 v56, v56\n\t" \
    "v_exp_f32 v57, v57\n\t" \
    "v_exp_f32 v58, v58\n\t" \
    "v_mfma_f32_32x32x16_bf16 v[16:31], v[112:115], v[90:93], v[16:31]\n\t" \
    "v_exp_f32 v59, v59\n\t" \
    "v_cvt_pk_bf16_f32 v94, v56, v57\n\t" \
    "v_cvt_pk_bf16_f32 v95, v58, v59\n\t" \
    "v_exp_f32 v60, v60\n\t" \
    "v_exp_f32 v61, v61\n\t" \
    "v_exp_f32 v62, v62\n\t" \
    "v_exp_f32 v63, v63\n\t" \
    "v_cvt_pk_bf16_f32 v96, v60, v61\n\t" \
    "v_cvt_pk_bf16_f32 v97, v62, v63\n\t" \
    "v_mfma_f32_32x32x8_bf16 v[48:63], v[80:81], v[74:75], 0\n\t" \
    "ds_read_b64 v[110:111], v99 offset:7488\n\t" \
    "ds_read_b128 v[106:109], v98 offset:7488\n\t" \
    "v_exp_f32 v32, v32\n\t" \
    "v_exp_f32 v33, v33\n\t" \
    "v_exp_f32 v34, v34\n\t" \
    "v_exp_f32 v35, v35\n\t" \
    "v_cvt_pk_bf16_f32 v90, v32, v33\n\t" \
    "v_cvt_pk_bf16_f32 v91, v34, v35\n\t" \
    "v_mfma_f32_32x32x16_bf16 v[16:31], v[86:89], v[94:97], v[16:31]\n\t" \
    "ds_read_b128 v[86:89], v100 offset:5600\n\t" \
    "ds_read_b128 v[112:115], v100 offset:7488\n\t" \
    "v_exp_f32 v36, v36\n\t" \
    "v_exp_f32 v37, v37\n\t" \
    "v_exp_f32 v38, v38\n\t" \
    "v_exp_f32 v39, v39\n\t" \
    "v_cvt_pk_bf16_f32 v92, v36, v37\n\t" \
    "v_cvt_pk_bf16_f32 v93, v38, v39\n\t" \
    "v_mfma_f32_32x32x16_bf16 v[48:63], v[76:79], v[70:73], v[48:63]\n\t" \
    "v_exp_f32 v40, v40\n\t" \
    "v_exp_f32 v41, v41\n\t" \
    "v_exp_f32 v42, v42\n\t" \
    "v_mfma_f32_32x32x16_bf16 v[0:15], v[82:85], v[90:93], v[0:15]\n\t" \
    "v_exp_f32 v43, v43\n\t" \
    "v_cvt_pk_bf16_f32 v94, v40, v41\n\t" \
    "v_cvt_pk_bf16_f32 v95, v42, v43\n\t" \
    "v_exp_f32 v44, v44\n\t" \
    "v_exp_f32 v45, v45\n\t" \
    "v_exp_f32 v46, v46\n\t" \
    "v_exp_f32 v47, v47\n\t" \
    "v_cvt_pk_bf16_f32 v96, v44, v45\n\t" \
    "v_cvt_pk_bf16_f32 v97, v46, v47\n\t" \
    "s_waitcnt lgkmcnt(0)\n\t" \
    "v_mfma_f32_32x32x8_bf16 v[32:47], v[110:111], v[68:69], 0\n\t" \
    "v_exp_f32 v48, v48\n\t" \
    "v_exp_f32 v49, v49\n\t" \
    "v_exp_f32 v50, v50\n\t" \
    "v_exp_f32 v51, v51\n\t" \
    "v_cvt_pk_bf16_f32 v90, v48, v49\n\t" \
    "v_cvt_pk_bf16_f32 v91, v50, v51\n\t" \
    "v_mfma_f32_32x32x16_bf16 v[0:15], v[86:89], v[94:97], v[0:15]\n\t" \
    "v_exp_f32 v52, v52\n\t" \
    "v_exp_f32 v53, v53\n\t" \
    "v_exp_f32 v54, v54\n\t" \
    "v_exp_f32 v55, v55\n\t" \
    "v_cvt_pk_bf16_f32 v92, v52, v53\n\t" \
    "v_cvt_pk_bf16_f32 v93, v54, v55\n\t" \
    "v_mfma_f32_32x32x16_bf16 v[32:47], v[106:109], v[64:67], v[32:47]\n\t" \
    "s_add_u32 m0, %[so], %[ldsb]\n\t" \
    "s_nop 0\n\t" \
    "buffer_load_dwordx4 v101, %[srd], %[so] offen lds\n\t" \
    "s_add_u32 %[so], %[so], 4096\n\t" \
    "v_exp_f32 v56, v56\n\t" \
    "v_exp_f32 v57, v57\n\t" \
    "v_exp_f32 v58, v58\n\t" \
    "v_mfma_f32_32x32x16_bf16 v[16:31], v[82:85], v[90:93], v[16:31]\n\t" \
    "v_exp_f32 v59, v59\n\t" \
    "v_cvt_pk_bf16_f32 v94, v56, v57\n\t" \
    "v_cvt_pk_bf16_f32 v95, v58, v59\n\t" \
    "v_exp_f32 v60, v60\n\t" \
    "v_exp_f32 v61, v61\n\t" \
    "v_exp_f32 v62, v62\n\t" \
    "v_exp_f32 v63, v63\n\t" \
    "v_cvt_pk_bf16_f32 v96, v60, v61\n\t" \
    "v_cvt_pk_bf16_f32 v97, v62, v63\n\t" \
    "v_mfma_f32_32x32x8_bf16 v[48:63], v[110:111], v[74:75], 0\n\t" \
    "ds_read_b64 v[80:81], v99 offset:9984\n\t" \
    "ds_read_b128 v[76:79], v98 offset:9984\n\t" \
    "v_exp_f32 v32, v32\n\t" \
    "v_exp_f32 v33, v33\n\t" \
    "v_exp_f32 v34, v34\n\t" \
    "v_exp_f32 v35, v35\n\t" \
    "v_cvt_pk_bf16_f32 v90, v32, v33\n\t" \
    "v_cvt_pk_bf16_f32 v91, v34, v35\n\t" \
    "v_mfma_f32_32x32x16_bf16 v[16:31], v[86:89], v[94:97], v[16:31]\n\t" \
    "ds_read_b128 v[86:89], v100 offset:8096\n\t" \
    "ds_read_b128 v[82:85], v100 offset:9984\n\t" \
    "v_exp_f32 v36, v36\n\t" \
    "v_exp_f32 v37, v37\n\t" \
    "v_exp_f32 v38, v38\n\t" \
    "v_exp_f32 v39, v39\n\t" \
    "v_cvt_pk_bf16_f32 v92, v36, v37\n\t" \
    "v_cvt_pk_bf16_f32 v93, v38, v39\n\t" \
    "v_mfma_f32_32x32x16_bf16 v[48:63], v[106:109], v[70:73], v[48:63]\n\t" \
    "v_exp_f32 v40, v40\n\t" \
    "v_exp_f32 v41, v41\n\t" \
    "v_exp_f32 v42, v42\n\t" \
    "v_mfma_f32_32x32x16_bf16 v[0:15], v[112:115], v[90:93], v[0:15]\n\t" \
    "v_exp_f32 v43, v43\n\t" \
    "v_cvt_pk_bf16_f32 v94, v40, v41\n\t" \
    "v_cvt_pk_bf16_f32 v95, v42, v43\n\t" \
    "v_exp_f32 v44, v44\n\t" \
    "v_exp_f32 v45, v45\n\t" \
    "v_exp_f32 v46, v46\n\t" \
    "v_exp_f32 v47, v47\n\t" \
    "v_cvt_pk_bf16_f32 v96, v44, v45\n\t" \
    "v_cvt_pk_bf16_f32 v97, v46, v47\n\t" \
    "s_waitcnt lgkmcnt(0)\n\t" \
    "v_mfma_f32_32x32x8_bf16 v[32:47], v[80:81], v[68:69], 0\n\t" \
    "v_exp_f32 v48, v48\n\t" \
    "v_exp_f32 v49, v49\n\t" \
    "v_exp_f32 v50, v50\n\t" \
    "v_exp_f32 v51, v51\n\t" \
    "v_cvt_pk_bf16_f32 v90, v48, v49\n\t" \
    "v_cvt_pk_bf16_f32 v91, v50, v51\n\t" \
    "v_mfma_f32_32x32x16_bf16 v[0:15], v[86:89], v[94:97], v[0:15]\n\t" \
    "v_exp_f32 v52, v52\n\t" \
    "v_exp_f32 v53, v53\n\t" \
    "v_exp_f32 v54, v54\n\t" \
    "v_exp_f32 v55, v55\n\t" \
    "v_cvt_pk_bf16_f32 v92, v52, v53\n\t" \
    "v_cvt_pk_bf16_f32 v93, v54, v55\n\t" \
    "v_mfma_f32_32x32x16_bf16 v[32:47], v[76:79], v[64:67], v[32:47]\n\t" \
    "s_add_u32 m0, %[so], %[ldsb]\n\t" \
    "s_nop 0\n\t" \
    "buffer_load_dwordx4 v101, %[srd], %[so] offen lds\n\t" \
    "s_add_u32 %[so], %[so], 4096\n\t" \
    "v_exp_f32 v56, v56\n\t" \
    "v_exp_f32 v57, v57\n\t" \
    "v_exp_f32 v58, v58\n\t" \
    "v_mfma_f32_32x32x16_bf16 v[16:31], v[112:115], v[90:93], v[16:31]\n\t" \
    "v_exp_f32 v59, v59\n\t" \
    "v_cvt_pk_bf16_f32 v94, v56, v57\n\t" \
    "v_cvt_pk_bf16_f32 v95, v58, v59\n\t" \
    "v_exp_f32 v60, v60\n\t" \
    "v_exp_f32 v61, v61\n\t" \
    "v_exp_f32 v62, v62\n\t" \
    "v_exp_f32 v63, v63\n\t" \
    "v_cvt_pk_bf16_f32 v96, v60, v61\n\t" \
    "v_cvt_pk_bf16_f32 v97, v62, v63\n\t" \
    "v_add_u32 v98, 9984, v98\n\t" \
    "v_add_u32 v99, 9984, v99\n\t" \
    "v_add_u32 v100, 9984, v100\n\t" \
    "s_waitcnt vmcnt(3)\n\t" \
    "s_barrier\n\t" \
    "s_cmp_eq_u32 %[pub], 0\n\t" \
    "s_cbranch_scc1 7f\n\t" \
    "s_mov_b64 exec, 1\n\t" \
    "v_mov_b32 v102, 0\n\t" \
    "v_mov_b32 v103, %[tag]\n\t" \
    "s_nop 0\n\t" \
    "global_store_dword v102, v103, %[flag] sc1\n\t" \
    "s_nop 1\n\t" \
    "s_mov_b64 exec, -1\n\t" \
    "7:\n\t" \
    "s_mov_b32 %[cnt], 2\n\t" \
    "1:\n\t" \
    "v_mfma_f32_32x32x8_bf16 v[48:63], v[80:81], v[74:75], 0\n\t" \
    "ds_read_b64 v[110:111], v99 offset:2496\n\t" \
    "ds_read_b128 v[106:109], v98 offset:2496\n\t" \
    "v_exp_f32 v32, v32\n\t" \
    "v_exp_f32 v33, v33\n\t" \
    "v_exp_f32 v34, v34\n\t" \
    "v_exp_f32 v35, v35\n\t" \
    "v_cvt_pk_bf16_f32 v90, v32, v33\n\t" \
    "v_cvt_pk_bf16_f32 v91, v34, v35\n\t" \
    "v_mfma_f32_32x32x16_bf16 v[16:31], v[86:89], v[94:97], v[16:31]\n\t" \
    "ds_read_b128 v[86:89], v100 offset:608\n\t" \
    "ds_read_b128 v[112:115], v100 offset:2496\n\t" \
    "v_exp_f32 v36, v36\n\t" \
    "v_exp_f32 v37, v37\n\t" \
    "v_exp_f32 v38, v38\n\t" \
    "v_exp_f32 v39, v39\n\t" \
    "v_cvt_pk_bf16_f32 v92, v36, v37\n\t" \
    "v_cvt_pk_bf16_f32 v93, v38, v39\n\t" \
    "v_mfma_f32_32x32x16_bf16 v[48:63], v[76:79], v[70:73], v[48:63]\n\t" \
    "v_exp_f32 v40, v40\n\t" \
    "v_exp_f32 v41, v41\n\t" \
    "v_exp_f32 v42, v42\n\t" \
    "v_mfma_f32_32x32x16_bf16 v[0:15], v[82:85], v[90:93], v[0:15]\n\t" \
    "v_exp_f32 v43, v43\n\t" \
    "v_cvt_pk_bf16_f32 v94, v40, v41\n\t" \
    "v_cvt_pk_bf16_f32 v95, v42, v43\n\t" \
    "v_exp_f32 v44, v44\n\t" \
    "v_exp_f32 v45, v45\n\t" \
    "v_exp_f32 v46, v46\n\t" \
    "v_exp_f32 v47, v47\n\t" \
    "v_cvt_pk_bf16_f32 v96, v44, v45\n\t" \
    "v_cvt_pk_bf16_f32 v97, v46, v47\n\t" \
    "s_waitcnt lgkmcnt(0)\n\t" \
    "v_mfma_f32_32x32x8_bf16 v[32:47], v[110:111], v[68:69], 0\n\t" \
    "v_exp_f32 v48, v48\n\t" \
    "v_exp_f32 v49, v49\n\t" \
    "v_exp_f32 v50, v50\n\t" \
    "v_exp_f32 v51, v51\n\t" \
    "v_cvt_pk_bf16_f32 v90, v48, v49\n\t" \
    "v_cvt_pk_bf16_f32 v91, v50, v51\n\t" \
    "v_mfma_f32_32x32x16_bf16 v[0:15], v[86:89], v[94:97], v[0:15]\n\t" \
    "v_exp_f32 v52, v52\n\t" \
    "v_exp_f32 v53, v53\n\t" \
    "v_exp_f32 v54, v54\n\t" \
    "v_exp_f32 v55, v55\n\t" \
    "v_cvt_pk_bf16_f32 v92, v52, v53\n\t" \
    "v_cvt_pk_bf16_f32 v93, v54, v55\n\t" \
    "v_mfma_f32_32x32x16_bf16 v[32:47], v[106:109], v[64:67], v[32:47]\n\t" \
    "s_add_u32 m0, %[so], %[ldsb]\n\t" \
    "s_nop 0\n\t" \
    "buffer_load_dwordx4 v101, %[srd], %[so] offen lds\n\t" \
    "s_add_u32 %[so], %[so], 4096\n\t" \
    "v_exp_f32 v56, v56\n\t" \
    "v_exp_f32 v57, v57\n\t" \
    "v_exp_f32 v58, v58\n\t" \
    "v_mfma_f32_32x32x16_bf16 v[16:31], v[82:85], v[90:93], v[16:31]\n\t" \
    "v_exp_f32 v59, v59\n\t" \
    "v_cvt_pk_bf16_f32 v94, v56, v57\n\t" \
    "v_cvt_pk_bf16_f32 v95, v58, v59\n\t" \
    "v_exp_f32 v60, v60\n\t" \
    "v_exp_f32 v61, v61\n\t" \
    "v_exp_f32 v62, v62\n\t" \
    "v_exp_f32 v63, v63\n\t" \
    "v_cvt_pk_bf16_f32 v96, v60, v61\n\t" \
    "v_cvt_pk_bf16_f32 v97, v62, v63\n\t" \
    "v_mfma_f32_32x32x8_bf16 v[48:63], v[110:111], v[74:75], 0\n\t" \
    "ds_read_b64 v[80:81], v99 offset:4992\n\t" \
    "ds_read_b128 v[76:79], v98 offset:4992\n\t" \
    "v_exp_f32 v32, v32\n\t" \
    "v_exp_f32 v33, v33\n\t" \
    "v_exp_f32 v34, v34\n\t" \
    "v_exp_f32 v35, v35\n\t" \
    "v_cvt_pk_bf16_f32 v90, v32, v33\n\t" \
    "v_cvt_pk_bf16_f32 v91, v34, v35\n\t" \
    "v_mfma_f32_32x32x16_bf16 v[16:31], v[86:89], v[94:97], v[16:31]\n\t" \
    "ds_read_b128 v[86:89], v100 offset:3104\n\t" \
    "ds_read_b128 v[82:85], v100 offset:4992\n\t" \
    "v_exp_f32 v36, v36\n\t" \
    "v_exp_f32 v37, v37\n\t" \
    "v_exp_f32 v38, v38\n\t" \
    "v_exp_f32 v39, v39\n\t" \
    "v_cvt_pk_bf16_f32 v92, v36, v37\n\t" \
    "v_cvt_pk_bf16_f32 v93, v38, v39\n\t" \
    "v_mfma_f32_32x32x16_bf16 v[48:63], v[106:109], v[70:73], v[48:63]\n\t" \
    "v_exp_f32 v40, v40\n\t" \
    "v_exp_f32 v41, v41\n\t" \
    "v_exp_f32 v42, v42\n\t" \
    "v_mfma_f32_32x32x16_bf16 v[0:15], v[112:115], v[90:93], v[0:15]\n\t" \
    "v_exp_f32 v43, v43\n\t" \
    "v_cvt_pk_bf16_f32 v94, v40, v41\n\t" \
    "v_cvt_pk_bf16_f32 v95, v42, v43\n\t" \
    "v_exp_f32 v44, v44\n\t" \
    "v_exp_f32 v45, v45\n\t" \
    "v_exp_f32 v46, v46\n\t" \
    "v_exp_f32 v47, v47\n\t" \
    "v_cvt_pk_bf16_f32 v96, v44, v45\n\t" \
    "v_cvt_pk_bf16_f32 v97, v46, v47\n\t" \
    "s_waitcnt lgkmcnt(0)\n\t" \
    "v_mfma_f32_32x32x8_bf16 v[32:47], v[80:81], v[68:69], 0\n\t" \
    "v_exp_f32 v48, v48\n\t" \
    "v_exp_f32 v49, v49\n\t" \
    "v_exp_f32 v50, v50\n\t" \
    "v_exp_f32 v51, v51\n\t" \
    "v_cvt_pk_bf16_f32 v90, v48, v49\n\t" \
    "v_cvt_pk_bf16_f32 v91, v50, v51\n\t" \
    "v_mfma_f32_32x32x16_bf16 v[0:15], v[86:89], v[94:97], v[0:15]\n\t" \
    "v_exp_f32 v52, v52\n\t" \
    "v_exp_f32 v53, v53\n\t" \
    "v_exp_f32 v54, v54\n\t" \
    "v_exp_f32 v55, v55\n\t" \
    "v_cvt_pk_bf16_f32 v92, v52, v53\n\t" \
    "v_cvt_pk_bf16_f32 v93, v54, v55\n\t" \
    "v_mfma_f32_32x32x16_bf16 v[32:47], v[76:79], v[64:67], v[32:47]\n\t" \
    "s_add_u32 m0, %[so], %[ldsb]\n\t" \
    "s_nop 0\n\t" \
    "buffer_load_dwordx4 v101, %[srd], %[so] offen lds\n\t" \
    "s_add_u32 %[so], %[so], 4096\n\t" \
    "v_exp_f32 v56, v56\n\t" \
    "v_exp_f32 v57, v57\n\t" \
    "v_exp_f32 v58, v58\n\t" \
    "v_mfma_f32_32x32x16_bf16 v[16:31], v[112:115], v[90:93], v[16:31]\n\t" \
    "v_exp_f32 v59, v59\n\t" \
    "v_cvt_pk_bf16_f32 v94, v56, v57\n\t" \
    "v_cvt_pk_bf16_f32 v95, v58, v59\n\t" \
    "v_exp_f32 v60, v60\n\t" \
    "v_exp_f32 v61, v61\n\t" \
    "v_exp_f32 v62, v62\n\t" \
    "v_exp_f32 v63, v63\n\t" \
    "v_cvt_pk_bf16_f32 v96, v60, v61\n\t" \
    "v_cvt_pk_bf16_f32 v97, v62, v63\n\t" \
    "v_mfma_f32_32x32x8_bf16 v[48:63], v[80:81], v[74:75], 0\n\t" \
    "ds_read_b64 v[110:111], v99 offset:7488\n\t" \
    "ds_read_b128 v[106:109], v98 offset:7488\n\t" \
    "v_exp_f32 v32, v32\n\t" \
    "v_exp_f32 v33, v33\n\t" \
    "v_exp_f32 v34, v34\n\t" \
    "v_exp_f32 v35, v35\n\t" \
    "v_cvt_pk_bf16_f32 v90, v32, v33\n\t" \
    "v_cvt_pk_bf16_f32 v91, v34, v35\n\t" \
    "v_mfma_f32_32x32x16_bf16 v[16:31], v[86:89], v[94:97], v[16:31]\n\t" \
    "ds_read_b128 v[86:89], v100 offset:5600\n\t" \
    "ds_read_b128 v[112:115], v100 offset:7488\n\t" \
    "v_exp_f32 v36, v36\n\t" \
    "v_exp_f32 v37, v37\n\t" \
    "v_exp_f32 v38, v38\n\t" \
    "v_exp_f32 v39, v39\n\t" \
    "v_cvt_pk_bf16_f32 v92, v36, v37\n\t" \
    "v_cvt_pk_bf16_f32 v93, v38, v39\n\t" \
    "v_mfma_f32_32x32x16_bf16 v[48:63], v[76:79], v[70:73], v[48:63]\n\t" \
    "v_exp_f32 v40, v40\n\t" \
    "v_exp_f32 v41, v41\n\t" \
    "v_exp_f32 v42, v42\n\t" \
    "v_mfma_f32_32x32x16_bf16 v[0:15], v[82:85], v[90:93], v[0:15]\n\t" \
    "v_exp_f32 v43, v43\n\t" \
    "v_cvt_pk_bf16_f32 v94, v40, v41\n\t" \
    "v_cvt_pk_bf16_f32 v95, v42, v43\n\t" \
    "v_exp_f32 v44, v44\n\t" \
    "v_exp_f32 v45, v45\n\t" \
    "v_exp_f32 v46, v46\n\t" \
    "v_exp_f32 v47, v47\n\t" \
    "v_cvt_pk_bf16_f32 v96, v44, v45\n\t" \
    "v_cvt_pk_bf16_f32 v97, v46, v47\n\t" \
    "s_waitcnt lgkmcnt(0)\n\t" \
    "v_mfma_f32_32x32x8_bf16 v[32:47], v[110:111], v[68:69], 0\n\t" \
    "v_exp_f32 v48, v48\n\t" \
    "v_exp_f32 v49, v49\n\t" \
    "v_exp_f32 v50, v50\n\t" \
    "v_exp_f32 v51, v51\n\t" \
    "v_cvt_pk_bf16_f32 v90, v48, v49\n\t" \
    "v_cvt_pk_bf16_f32 v91, v50, v51\n\t" \
    "v_mfma_f32_32x32x16_bf16 v[0:15], v[86:89], v[94:97], v[0:15]\n\t" \
    "v_exp_f32 v52, v52\n\t" \
    "v_exp_f32 v53, v53\n\t" \
    "v_exp_f32 v54, v54\n\t" \
    "v_exp_f32 v55, v55\n\t" \
    "v_cvt_pk_bf16_f32 v92, v52, v53\n\t" \
    "v_cvt_pk_bf16_f32 v93, v54, v55\n\t" \
    "v_mfma_f32_32x32x16_bf16 v[32:47], v[106:109], v[64:67], v[32:47]\n\t" \
    "s_add_u32 m0, %[so], %[ldsb]\n\t" \
    "s_nop 0\n\t" \
    "buffer_load_dwordx4 v101, %[srd], %[so] offen lds\n\t" \
    "s_add_u32 %[so], %[so], 4096\n\t" \
    "v_exp_f32 v56, v56\n\t" \
    "v_exp_f32 v57, v57\n\t" \
    "v_exp_f32 v58, v58\n\t" \
    "v_mfma_f32_32x32x16_bf16 v[16:31], v[82:85], v[90:93], v[16:31]\n\t" \
    "v_exp_f32 v59, v59\n\t" \
    "v_cvt_pk_bf16_f32 v94, v56, v57\n\t" \
    "v_cvt_pk_bf16_f32 v95, v58, v59\n\t" \
    "v_exp_f32 v60, v60\n\t" \
    "v_exp_f32 v61, v61\n\t" \
    "v_exp_f32 v62, v62\n\t" \
    "v_exp_f32 v63, v63\n\t" \
    "v_cvt_pk_bf16_f32 v96, v60, v61\n\t" \
    "v_cvt_pk_bf16_f32 v97, v62, v63\n\t" \
    "v_mfma_f32_32x32x8_bf16 v[48:63], v[110:111], v[74:75], 0\n\t" \
    "ds_read_b64 v[80:81], v99 offset:9984\n\t" \
    "ds_read_b128 v[76:79], v98 offset:9984\n\t" \
    "v_exp_f32 v32, v32\n\t" \
    "v_exp_f32 v33, v33\n\t" \
    "v_exp_f32 v34, v34\n\t" \
    "v_exp_f32 v35, v35\n\t" \
    "v_cvt_pk_bf16_f32 v90, v32, v33\n\t" \
    "v_cvt_pk_bf16_f32 v91, v34, v35\n\t" \
    "v_mfma_f32_32x32x16_bf16 v[16:31], v[86:89], v[94:97], v[16:31]\n\t" \
    "ds_read_b128 v[86:89], v100 offset:8096\n\t" \
    "ds_read_b128 v[82:85], v100 offset:9984\n\t" \
    "v_exp_f32 v36, v36\n\t" \
    "v_exp_f32 v37, v37\n\t" \
    "v_exp_f32 v38, v38\n\t" \
    "v_exp_f32 v39, v39\n\t" \
    "v_cvt_pk_bf16_f32 v92, v36, v37\n\t" \
    "v_cvt_pk_bf16_f32 v93, v38, v39\n\t" \
    "v_mfma_f32_32x32x16_bf16 v[48:63], v[106:109], v[70:73], v[48:63]\n\t" \
    "v_exp_f32 v40, v40\n\t" \
    "v_exp_f32 v41, v41\n\t" \
    "v_exp_f32 v42, v42\n\t" \
    "v_mfma_f32_32x32x16_bf16 v[0:15], v[112:115], v[90:93], v[0:15]\n\t" \
    "v_exp_f32 v43, v43\n\t" \
    "v_cvt_pk_bf16_f32 v94, v40, v41\n\t" \
    "v_cvt_pk_bf16_f32 v95, v42, v43\n\t" \
    "v_exp_f32 v44, v44\n\t" \
    "v_exp_f32 v45, v45\n\t" \
    "v_exp_f32 v46, v46\n\t" \
    "v_exp_f32 v47, v47\n\t" \
    "v_cvt_pk_bf16_f32 v96, v44, v45\n\t" \
    "v_cvt_pk_bf16_f32 v97, v46, v47\n\t" \
    "s_waitcnt lgkmcnt(0)\n\t" \
    "v_mfma_f32_32x32x8_bf16 v[32:47], v[80:81], v[68:69], 0\n\t" \
    "v_exp_f32 v48, v48\n\t" \
    "v_exp_f32 v49, v49\n\t" \
    "v_exp_f32 v50, v50\n\t" \
    "v_exp_f32 v51, v51\n\t" \
    "v_cvt_pk_bf16_f32 v90, v48, v49\n\t" \
    "v_cvt_pk_bf16_f32 v91, v50, v51\n\t" \
    "v_mfma_f32_32x32x16_bf16 v[0:15], v[86:89], v[94:97], v[0:15]\n\t" \
    "v_exp_f32 v52, v52\n\t" \
    "v_exp_f32 v53, v53\n\t" \
    "v_exp_f32 v54, v54\n\t" \
    "v_exp_f32 v55, v55\n\t" \
    "v_cvt_pk_bf16_f32 v92, v52, v53\n\t" \
    "v_cvt_pk_bf16_f32 v93, v54, v55\n\t" \
    "v_mfma_f32_32x32x16_bf16 v[32:47], v[76:79], v[64:67], v[32:47]\n\t" \
    "s_add_u32 m0, %[so], %[ldsb]\n\t" \
    "s_nop 0\n\t" \
    "buffer_load_dwordx4 v101, %[srd], %[so] offen lds\n\t" \
    "s_add_u32 %[so], %[so], 4096\n\t" \
    "v_exp_f32 v56, v56\n\t" \
    "v_exp_f32 v57, v57\n\t" \
    "v_exp_f32 v58, v58\n\t" \
    "v_mfma_f32_32x32x16_bf16 v[16:31], v[112:115], v[90:93], v[16:31]\n\t" \
    "v_exp_f32 v59, v59\n\t" \
    "v_cvt_pk_bf16_f32 v94, v56, v57\n\t" \
    "v_cvt_pk_bf16_f32 v95, v58, v59\n\t" \
    "v_exp_f32 v60, v60\n\t" \
    "v_exp_f32 v61, v61\n\t" \
    "v_exp_f32 v62, v62\n\t" \
    "v_exp_f32 v63, v63\n\t" \
    "v_cvt_pk_bf16_f32 v96, v60, v61\n\t" \
    "v_cvt_pk_bf16_f32 v97, v62, v63\n\t" \
    "v_add_u32 v98, 9984, v98\n\t" \
    "v_add_u32 v99, 9984, v99\n\t" \
    "v_add_u32 v100, 9984, v100\n\t" \
    "s_sub_u32 %[cnt], %[cnt], 1\n\t" \
    "s_cmp_lg_u32 %[cnt], 0\n\t" \
    "s_cbranch_scc1 1b\n\t" \
    "s_waitcnt vmcnt(7)\n\t" \
    "s_barrier\n\t" \
    ""
#define ATT8_B \
    "s_mov_b32 %[cnt], 2\n\t" \
    "2:\n\t" \
    "v_mfma_f32_32x32x8_bf16 v[48:63], v[80:81], v[74:75], 0\n\t" \
    "ds_read_b64 v[110:111], v99 offset:2496\n\t" \
    "ds_read_b128 v[106:109], v98 offset:2496\n\t" \
    "v_exp_f32 v32, v32\n\t" \
    "v_exp_f32 v33, v33\n\t" \
    "v_exp_f32 v34, v34\n\t" \
    "v_exp_f32 v35, v35\n\t" \
    "v_cvt_pk_bf16_f32 v90, v32, v33\n\t" \
    "v_cvt_pk_bf16_f32 v91, v34, v35\n\t" \
    "v_mfma_f32_32x32x16_bf16 v[16:31], v[86:89], v[94:97], v[16:31]\n\t" \
    "ds_read_b128 v[86:89], v100 offset:608\n\t" \
    "ds_read_b128 v[112:115], v100 offset:2496\n\t" \
    "v_exp_f32 v36, v36\n\t" \
    "v_exp_f32 v37, v37\n\t" \
    "v_exp_f32 v38, v38\n\t" \
    "v_exp_f32 v39, v39\n\t" \
    "v_cvt_pk_bf16_f32 v92, v36, v37\n\t" \
    "v_cvt_pk_bf16_f32 v93, v38, v39\n\t" \
    "v_mfma_f32_32x32x16_bf16 v[48:63], v[76:79], v[70:73], v[48:63]\n\t" \
    "v_exp_f32 v40, v40\n\t" \
    "v_exp_f32 v41, v41\n\t" \
    "v_exp_f32 v42, v42\n\t" \
    "v_mfma_f32_32x32x16_bf16 v[0:15], v[82:85], v[90:93], v[0:15]\n\t" \
    "v_exp_f32 v43, v43\n\t" \
    "v_cvt_pk_bf16_f32 v94, v40, v41\n\t" \
    "v_cvt_pk_bf16_f32 v95, v42, v43\n\t" \
    "v_exp_f32 v44, v44\n\t" \
    "v_exp_f32 v45, v45\n\t" \
    "v_exp_f32 v46, v46\n\t" \
    "v_exp_f32 v47, v47\n\t" \
    "v_cvt_pk_bf16_f32 v96, v44, v45\n\t" \
    "v_cvt_pk_bf16_f32 v97, v46, v47\n\t" \
    "s_waitcnt lgkmcnt(0)\n\t" \
    "v_mfma_f32_32x32x8_bf16 v[32:47], v[110:111], v[68:69], 0\n\t" \
    "v_exp_f32 v48, v48\n\t" \
    "v_exp_f32 v49, v49\n\t" \
    "v_exp_f32 v50, v50\n\t" \
    "v_exp_f32 v51, v51\n\t" \
    "v_cvt_pk_bf16_f32 v90, v48, v49\n\t" \
    "v_cvt_pk_bf16_f32 v91, v50, v51\n\t" \
    "v_mfma_f32_32x32x16_bf16 v[0:15], v[86:89], v[94:97], v[0:15]\n\t" \
    "v_exp_f32 v52, v52\n\t" \
    "v_exp_f32 v53, v53\n\t" \
    "v_exp_f32 v54, v54\n\t" \
    "v_exp_f32 v55, v55\n\t" \
    "v_cvt_pk_bf16_f32 v92, v52, v53\n\t" \
    "v_cvt_pk_bf16_f32 v93, v54, v55\n\t" \
    "v_mfma_f32_32x32x16_bf16 v[32:47], v[106:109], v[64:67], v[32:47]\n\t" \
    "v_exp_f32 v56, v56\n\t" \
    "v_exp_f32 v57, v57\n\t" \
    "v_exp_f32 v58, v58\n\t" \
    "v_mfma_f32_32x32x16_bf16 v[16:31], v[82:85], v[90:93], v[16:31]\n\t" \
    "v_exp_f32 v59, v59\n\t" \
    "v_cvt_pk_bf16_f32 v94, v56, v57\n\t" \
    "v_cvt_pk_bf16_f32 v95, v58, v59\n\t" \
    "v_exp_f32 v60, v60\n\t" \
    "v_exp_f32 v61, v61\n\t" \
    "v_exp_f32 v62, v62\n\t" \
    "v_exp_f32 v63, v63\n\t" \
    "v_cvt_pk_bf16_f32 v96, v60, v61\n\t" \
    "v_cvt_pk_bf16_f32 v97, v62, v63\n\t" \
    "v_mfma_f32_32x32x8_bf16 v[48:63], v[110:111], v[74:75], 0\n\t" \
    "ds_read_b64 v[80:81], v99 offset:4992\n\t" \
    "ds_read_b128 v[76:79], v98 offset:4992\n\t" \
    "v_exp_f32 v32, v32\n\t" \
    "v_exp_f32 v33, v33\n\t" \
    "v_exp_f32 v34, v34\n\t" \
    "v_exp_f32 v35, v35\n\t" \
    "v_cvt_pk_bf16_f32 v90, v32, v33\n\t" \
    "v_cvt_pk_bf16_f32 v91, v34, v35\n\t" \
    "v_mfma_f32_32x32x16_bf16 v[16:31], v[86:89], v[94:97], v[16:31]\n\t" \
    "ds_read_b128 v[86:89], v100 offset:3104\n\t" \
    "ds_read_b128 v[82:85], v100 offset:4992\n\t" \
    "v_exp_f32 v36, v36\n\t" \
    "v_exp_f32 v37, v37\n\t" \
    "v_exp_f32 v38, v38\n\t" \
    "v_exp_f32 v39, v39\n\t" \
    "v_cvt_pk_bf16_f32 v92, v36, v37\n\t" \
    "v_cvt_pk_bf16_f32 v93, v38, v39\n\t" \
    "v_mfma_f32_32x32x16_bf16 v[48:63], v[106:109], v[70:73], v[48:63]\n\t" \
    "v_exp_f32 v40, v40\n\t" \
    "v_exp_f32 v41, v41\n\t" \
    "v_exp_f32 v42, v42\n\t" \
    "v_mfma_f32_32x32x16_bf16 v[0:15], v[112:115], v[90:93], v[0:15]\n\t" \
    "v_exp_f32 v43, v43\n\t" \
    "v_cvt_pk_bf16_f32 v94, v40, v41\n\t" \
    "v_cvt_pk_bf16_f32 v95, v42, v43\n\t" \
    "v_exp_f32 v44, v44\n\t" \
    "v_exp_f32 v45, v45\n\t" \
    "v_exp_f32 v46, v46\n\t" \
    "v_exp_f32 v47, v47\n\t" \
    "v_cvt_pk_bf16_f32 v96, v44, v45\n\t" \
    "v_cvt_pk_bf16_f32 v97, v46, v47\n\t" \
    "s_waitcnt lgkmcnt(0)\n\t" \
    "v_mfma_f32_32x32x8_bf16 v[32:47], v[80:81], v[68:69], 0\n\t" \
    "v_exp_f32 v48, v48\n\t" \
    "v_exp_f32 v49, v49\n\t" \
    "v_exp_f32 v50, v50\n\t" \
    "v_exp_f32 v51, v51\n\t" \
    "v_cvt_pk_bf16_f32 v90, v48, v49\n\t" \
    "v_cvt_pk_bf16_f32 v91, v50, v51\n\t" \
    "v_mfma_f32_32x32x16_bf16 v[0:15], v[86:89], v[94:97], v[0:15]\n\t" \
    "v_exp_f32 v52, v52\n\t" \
    "v_exp_f32 v53, v53\n\t" \
    "v_exp_f32 v54, v54\n\t" \
    "v_exp_f32 v55, v55\n\t" \
    "v_cvt_pk_bf16_f32 v92, v52, v53\n\t" \
    "v_cvt_pk_bf16_f32 v93, v54, v55\n\t" \
    "v_mfma_f32_32x32x16_bf16 v[32:47], v[76:79], v[64:67], v[32:47]\n\t" \
    "v_exp_f32 v56, v56\n\t" \
    "v_exp_f32 v57, v57\n\t" \
    "v_exp_f32 v58, v58\n\t" \
    "v_mfma_f32_32x32x16_bf16 v[16:31], v[112:115], v[90:93], v[16:31]\n\t" \
    "v_exp_f32 v59, v59\n\t" \
    "v_cvt_pk_bf16_f32 v94, v56, v57\n\t" \
    "v_cvt_pk_bf16_f32 v95, v58, v59\n\t" \
    "v_exp_f32 v60, v60\n\t" \
    "v_exp_f32 v61, v61\n\t" \
    "v_exp_f32 v62, v62\n\t" \
    "v_exp_f32 v63, v63\n\t" \
    "v_cvt_pk_bf16_f32 v96, v60, v61\n\t" \
    "v_cvt_pk_bf16_f32 v97, v62, v63\n\t" \
    "v_mfma_f32_32x32x8_bf16 v[48:63], v[80:81], v[74:75], 0\n\t" \
    "ds_read_b64 v[110:111], v99 offset:7488\n\t" \
    "ds_read_b128 v[106:109], v98 offset:7488\n\t" \
    "v_exp_f32 v32, v32\n\t" \
    "v_exp_f32 v33, v33\n\t" \
    "v_exp_f32 v34, v34\n\t" \
    "v_exp_f32 v35, v35\n\t" \
    "v_cvt_pk_bf16_f32 v90, v32, v33\n\t" \
    "v_cvt_pk_bf16_f32 v91, v34, v35\n\t" \
    "v_mfma_f32_32x32x16_bf16 v[16:31], v[86:89], v[94:97], v[16:31]\n\t" \
    "ds_read_b128 v[86:89], v100 offset:5600\n\t" \
    "ds_read_b128 v[112:115], v100 offset:7488\n\t" \
    "v_exp_f32 v36, v36\n\t" \
    "v_exp_f32 v37, v37\n\t" \
    "v_exp_f32 v38, v38\n\t" \
    "v_exp_f32 v39, v39\n\t" \
    "v_cvt_pk_bf16_f32 v92, v36, v37\n\t" \
    "v_cvt_pk_bf16_f32 v93, v38, v39\n\t" \
    "v_mfma_f32_32x32x16_bf16 v[48:63], v[76:79], v[70:73], v[48:63]\n\t" \
    "v_exp_f32 v40, v40\n\t" \
    "v_exp_f32 v41, v41\n\t" \
    "v_exp_f32 v42, v42\n\t" \
    "v_mfma_f32_32x32x16_bf16 v[0:15], v[82:85], v[90:93], v[0:15]\n\t" \
    "v_exp_f32 v43, v43\n\t" \
    "v_cvt_pk_bf16_f32 v94, v40, v41\n\t" \
    "v_cvt_pk_bf16_f32 v95, v42, v43\n\t" \
    "v_exp_f32 v44, v44\n\t" \
    "v_exp_f32 v45, v45\n\t" \
    "v_exp_f32 v46, v46\n\t" \
    "v_exp_f32 v47, v47\n\t" \
    "v_cvt_pk_bf16_f32 v96, v44, v45\n\t" \
    "v_cvt_pk_bf16_f32 v97, v46, v47\n\t" \
    "s_waitcnt lgkmcnt(0)\n\t" \
    "v_mfma_f32_32x32x8_bf16 v[32:47], v[110:111], v[68:69], 0\n\t" \
    "v_exp_f32 v48, v48\n\t" \
    "v_exp_f32 v49, v49\n\t" \
    "v_exp_f32 v50, v50\n\t" \
    "v_exp_f32 v51, v51\n\t" \
    "v_cvt_pk_bf16_f32 v90, v48, v49\n\t" \
    "v_cvt_pk_bf16_f32 v91, v50, v51\n\t" \
    "v_mfma_f32_32x32x16_bf16 v[0:15], v[86:89], v[94:97], v[0:15]\n\t" \
    "v_exp_f32 v52, v52\n\t" \
    "v_exp_f32 v53, v53\n\t" \
    "v_exp_f32 v54, v54\n\t" \
    "v_exp_f32 v55, v55\n\t" \
    "v_cvt_pk_bf16_f32 v92, v52, v53\n\t" \
    "v_cvt_pk_bf16_f32 v93, v54, v55\n\t" \
    "v_mfma_f32_32x32x16_bf16 v[32:47], v[106:109], v[64:67], v[32:47]\n\t" \
    "v_exp_f32 v56, v56\n\t" \
    "v_exp_f32 v57, v57\n\t" \
    "v_exp_f32 v58, v58\n\t" \
    "v_mfma_f32_32x32x16_bf16 v[16:31], v[82:85], v[90:93], v[16:31]\n\t" \
    "v_exp_f32 v59, v59\n\t" \
    "v_cvt_pk_bf16_f32 v94, v56, v57\n\t" \
    "v_cvt_pk_bf16_f32 v95, v58, v59\n\t" \
    "v_exp_f32 v60, v60\n\t" \
    "v_exp_f32 v61, v61\n\t" \
    "v_exp_f32 v62, v62\n\t" \
    "v_exp_f32 v63, v63\n\t" \
    "v_cvt_pk_bf16_f32 v96, v60, v61\n\t" \
    "v_cvt_pk_bf16_f32 v97, v62, v63\n\t" \
    "v_mfma_f32_32x32x8_bf16 v[48:63], v[110:111], v[74:75], 0\n\t" \
    "ds_read_b64 v[80:81], v99 offset:9984\n\t" \
    "ds_read_b128 v[76:79], v98 offset:9984\n\t" \
    "v_exp_f32 v32, v32\n\t" \
    "v_exp_f32 v33, v33\n\t" \
    "v_exp_f32 v34, v34\n\t" \
    "v_exp_f32 v35, v35\n\t" \
    "v_cvt_pk_bf16_f32 v90, v32, v33\n\t" \
    "v_cvt_pk_bf16_f32 v91, v34, v35\n\t" \
    "v_mfma_f32_32x32x16_bf16 v[16:31], v[86:89], v[94:97], v[16:31]\n\t" \
    "ds_read_b128 v[86:89], v100 offset:8096\n\t" \
    "ds_read_b128 v[82:85], v100 offset:9984\n\t" \
    "v_exp_f32 v36, v36\n\t" \
    "v_exp_f32 v37, v37\n\t" \
    "v_exp_f32 v38, v38\n\t" \
    "v_exp_f32 v39, v39\n\t" \
    "v_cvt_pk_bf16_f32 v92, v36, v37\n\t" \
    "v_cvt_pk_bf16_f32 v93, v38, v39\n\t" \
    "v_mfma_f32_32x32x16_bf16 v[48:63], v[106:109], v[70:73], v[48:63]\n\t" \
    "v_exp_f32 v40, v40\n\t" \
    "v_exp_f32 v41, v41\n\t" \
    "v_exp_f32 v42, v42\n\t" \
    "v_mfma_f32_32x32x16_bf16 v[0:15], v[112:115], v[90:93], v[0:15]\n\t" \
    "v_exp_f32 v43, v43\n\t" \
    "v_cvt_pk_bf16_f32 v94, v40, v41\n\t" \
    "v_cvt_pk_bf16_f32 v95, v42, v43\n\t" \
    "v_exp_f32 v44, v44\n\t" \
    "v_exp_f32 v45, v45\n\t" \
    "v_exp_f32 v46, v46\n\t" \
    "v_exp_f32 v47, v47\n\t" \
    "v_cvt_pk_bf16_f32 v96, v44, v45\n\t" \
    "v_cvt_pk_bf16_f32 v97, v46, v47\n\t" \
    "s_waitcnt lgkmcnt(0)\n\t" \
    "v_mfma_f32_32x32x8_bf16 v[32:47], v[80:81], v[68:69], 0\n\t" \
    "v_exp_f32 v48, v48\n\t" \
    "v_exp_f32 v49, v49\n\t" \
    "v_exp_f32 v50, v50\n\t" \
    "v_exp_f32 v51, v51\n\t" \
    "v_cvt_pk_bf16_f32 v90, v48, v49\n\t" \
    "v_cvt_pk_bf16_f32 v91, v50, v51\n\t" \
    "v_mfma_f32_32x32x16_bf16 v[0:15], v[86:89], v[94:97], v[0:15]\n\t" \
    "v_exp_f32 v52, v52\n\t" \
    "v_exp_f32 v53, v53\n\t" \
    "v_exp_f32 v54, v54\n\t" \
    "v_exp_f32 v55, v55\n\t" \
    "v_cvt_pk_bf16_f32 v92, v52, v53\n\t" \
    "v_cvt_pk_bf16_f32 v93, v54, v55\n\t" \
    "v_mfma_f32_32x32x16_bf16 v[32:47], v[76:79], v[64:67], v[32:47]\n\t" \
    "v_exp_f32 v56, v56\n\t" \
    "v_exp_f32 v57, v57\n\t" \
    "v_exp_f32 v58, v58\n\t" \
    "v_mfma_f32_32x32x16_bf16 v[16:31], v[112:115], v[90:93], v[16:31]\n\t" \
    "v_exp_f32 v59, v59\n\t" \
    "v_cvt_pk_bf16_f32 v94, v56, v57\n\t" \
    "v_cvt_pk_bf16_f32 v95, v58, v59\n\t" \
    "v_exp_f32 v60, v60\n\t" \
    "v_exp_f32 v61, v61\n\t" \
    "v_exp_f32 v62, v62\n\t" \
    "v_exp_f32 v63, v63\n\t" \
    "v_cvt_pk_bf16_f32 v96, v60, v61\n\t" \
    "v_cvt_pk_bf16_f32 v97, v62, v63\n\t" \
    "v_add_u32 v98, 9984, v98\n\t" \
    "v_add_u32 v99, 9984, v99\n\t" \
    "v_add_u32 v100, 9984, v100\n\t" \
    "s_sub_u32 %[cnt], %[cnt], 1\n\t" \
    "s_cmp_lg_u32 %[cnt], 0\n\t" \
    "s_cbranch_scc1 2b\n\t" \
    "s_waitcnt vmcnt(0)\n\t" \
    "s_barrier\n\t" \
    "s_mov_b32 %[cnt], 2\n\t" \
    "3:\n\t" \
    "v_mfma_f32_32x32x8_bf16 v[48:63], v[80:81], v[74:75], 0\n\t" \
    "ds_read_b64 v[110:111], v99 offset:2496\n\t" \
    "ds_read_b128 v[106:109], v98 offset:2496\n\t" \
    "v_exp_f32 v32, v32\n\t" \
    "v_exp_f32 v33, v33\n\t" \
    "v_exp_f32 v34, v34\n\t" \
    "v_exp_f32 v35, v35\n\t" \
    "v_cvt_pk_bf16_f32 v90, v32, v33\n\t" \
    "v_cvt_pk_bf16_f32 v91, v34, v35\n\t" \
    "v_mfma_f32_32x32x16_bf16 v[16:31], v[86:89], v[94:97], v[16:31]\n\t" \
    "ds_read_b128 v[86:89], v100 offset:608\n\t" \
    "ds_read_b128 v[112:115], v100 offset:2496\n\t" \
    "v_exp_f32 v36, v36\n\t" \
    "v_exp_f32 v37, v37\n\t" \
    "v_exp_f32 v38, v38\n\t" \
    "v_exp_f32 v39, v39\n\t" \
    "v_cvt_pk_bf16_f32 v92, v36, v37\n\t" \
    "v_cvt_pk_bf16_f32 v93, v38, v39\n\t" \
    "v_mfma_f32_32x32x16_bf16 v[48:63], v[76:79], v[70:73], v[48:63]\n\t" \
    "v_exp_f32 v40, v40\n\t" \
    "v_exp_f32 v41, v41\n\t" \
    "v_exp_f32 v42, v42\n\t" \
    "v_mfma_f32_32x32x16_bf16 v[0:15], v[82:85], v[90:93], v[0:15]\n\t" \
    "v_exp_f32 v43, v43\n\t" \
    "v_cvt_pk_bf16_f32 v94, v40, v41\n\t" \
    "v_cvt_pk_bf16_f32 v95, v42, v43\n\t" \
    "v_exp_f32 v44, v44\n\t" \
    "v_exp_f32 v45, v45\n\t" \
    "v_exp_f32 v46, v46\n\t" \
    "v_exp_f32 v47, v47\n\t" \
    "v_cvt_pk_bf16_f32 v96, v44, v45\n\t" \
    "v_cvt_pk_bf16_f32 v97, v46, v47\n\t" \
    "s_waitcnt lgkmcnt(0)\n\t" \
    "v_mfma_f32_32x32x8_bf16 v[32:47], v[110:111], v[68:69], 0\n\t" \
    "v_exp_f32 v48, v48\n\t" \
    "v_exp_f32 v49, v49\n\t" \
    "v_exp_f32 v50, v50\n\t" \
    "v_exp_f32 v51, v51\n\t" \
    "v_cvt_pk_bf16_f32 v90, v48, v49\n\t" \
    "v_cvt_pk_bf16_f32 v91, v50, v51\n\t" \
    "v_mfma_f32_32x32x16_bf16 v[0:15], v[86:89], v[94:97], v[0:15]\n\t" \
    "v_exp_f32 v52, v52\n\t" \
    "v_exp_f32 v53, v53\n\t" \
    "v_exp_f32 v54, v54\n\t" \
    "v_exp_f32 v55, v55\n\t" \
    "v_cvt_pk_bf16_f32 v92, v52, v53\n\t" \
    "v_cvt_pk_bf16_f32 v93, v54, v55\n\t" \
    "v_mfma_f32_32x32x16_bf16 v[32:47], v[106:109], v[64:67], v[32:47]\n\t" \
    "s_min_u32 s94, %[wrow], 281\n\t" \
    "s_mul_i32 s95, s94, 144\n\t" \
    "s_add_u32 m0, s95, %[wlds]\n\t" \
    "s_mul_i32 s94, s94, 1152\n\t" \
    "s_add_u32 s96, %[wsl], s94\n\t" \
    "s_addc_u32 s97, %[wsh], 0\n\t" \
    "s_add_u32 %[wrow], %[wrow], 28\n\t" \
    "s_bitset0_b64 exec, 63\n\t" \
    "global_load_lds_dwordx4 v104, s[96:97]\n\t" \
    "s_mov_b64 exec, -1\n\t" \
    "v_exp_f32 v56, v56\n\t" \
    "v_exp_f32 v57, v57\n\t" \
    "v_exp_f32 v58, v58\n\t" \
    "v_mfma_f32_32x32x16_bf16 v[16:31], v[82:85], v[90:93], v[16:31]\n\t" \
    "v_exp_f32 v59, v59\n\t" \
    "v_cvt_pk_bf16_f32 v94, v56, v57\n\t" \
    "v_cvt_pk_bf16_f32 v95, v58, v59\n\t" \
    "v_exp_f32 v60, v60\n\t" \
    "v_exp_f32 v61, v61\n\t" \
    "v_exp_f32 v62, v62\n\t" \
    "v_exp_f32 v63, v63\n\t" \
    "v_cvt_pk_bf16_f32 v96, v60, v61\n\t" \
    "v_cvt_pk_bf16_f32 v97, v62, v63\n\t" \
    "v_mfma_f32_32x32x8_bf16 v[48:63], v[110:111], v[74:75], 0\n\t" \
    "ds_read_b64 v[80:81], v99 offset:4992\n\t" \
    "ds_read_b128 v[76:79], v98 offset:4992\n\t" \
    "v_exp_f32 v32, v32\n\t" \
    "v_exp_f32 v33, v33\n\t" \
    "v_exp_f32 v34, v34\n\t" \
    "v_exp_f32 v35, v35\n\t" \
    "v_cvt_pk_bf16_f32 v90, v32, v33\n\t" \
    "v_cvt_pk_bf16_f32 v91, v34, v35\n\t" \
    "v_mfma_f32_32x32x16_bf16 v[16:31], v[86:89], v[94:97], v[16:31]\n\t" \
    "ds_read_b128 v[86:89], v100 offset:3104\n\t" \
    "ds_read_b128 v[82:85], v100 offset:4992\n\t" \
    "v_exp_f32 v36, v36\n\t" \
    "v_exp_f32 v37, v37\n\t" \
    "v_exp_f32 v38, v38\n\t" \
    "v_exp_f32 v39, v39\n\t" \
    "v_cvt_pk_bf16_f32 v92, v36, v37\n\t" \
    "v_cvt_pk_bf16_f32 v93, v38, v39\n\t" \
    "v_mfma_f32_32x32x16_bf16 v[48:63], v[106:109], v[70:73], v[48:63]\n\t" \
    "v_exp_f32 v40, v40\n\t" \
    "v_exp_f32 v41, v41\n\t" \
    "v_exp_f32 v42, v42\n\t" \
    "v_mfma_f32_32x32x16_bf16 v[0:15], v[112:115], v[90:93], v[0:15]\n\t" \
    "v_exp_f32 v43, v43\n\t" \
    "v_cvt_pk_bf16_f32 v94, v40, v41\n\t" \
    "v_cvt_pk_bf16_f32 v95, v42, v43\n\t" \
    "v_exp_f32 v44, v44\n\t" \
    "v_exp_f32 v45, v45\n\t" \
    "v_exp_f32 v46, v46\n\t" \
    "v_exp_f32 v47, v47\n\t" \
    "v_cvt_pk_bf16_f32 v96, v44, v45\n\t" \
    "v_cvt_pk_bf16_f32 v97, v46, v47\n\t" \
    "s_waitcnt lgkmcnt(0)\n\t" \
    "v_mfma_f32_32x32x8_bf16 v[32:47], v[80:81], v[68:69], 0\n\t" \
    "v_exp_f32 v48, v48\n\t" \
    "v_exp_f32 v49, v49\n\t" \
    "v_exp_f32 v50, v50\n\t" \
    "v_exp_f32 v51, v51\n\t" \
    "v_cvt_pk_bf16_f32 v90, v48, v49\n\t" \
    "v_cvt_pk_bf16_f32 v91, v50, v51\n\t" \
    "v_mfma_f32_32x32x16_bf16 v[0:15], v[86:89], v[94:97], v[0:15]\n\t" \
    "v_exp_f32 v52, v52\n\t" \
    "v_exp_f32 v53, v53\n\t" \
    "v_exp_f32 v54, v54\n\t" \
    "v_exp_f32 v55, v55\n\t" \
    "v_cvt_pk_bf16_f32 v92, v52, v53\n\t" \
    "v_cvt_pk_bf16_f32 v93, v54, v55\n\t" \
    "v_mfma_f32_32x32x16_bf16 v[32:47], v[76:79], v[64:67], v[32:47]\n\t" \
    "s_min_u32 s94, %[wrow], 281\n\t" \
    "s_mul_i32 s95, s94, 144\n\t" \
    "s_add_u32 m0, s95, %[wlds]\n\t" \
    "s_mul_i32 s94, s94, 1152\n\t" \
    "s_add_u32 s96, %[wsl], s94\n\t" \
    "s_addc_u32 s97, %[wsh], 0\n\t" \
    "s_add_u32 %[wrow], %[wrow], 28\n\t" \
    "s_bitset0_b64 exec, 63\n\t" \
    "global_load_lds_dwordx4 v104, s[96:97]\n\t" \
    "s_mov_b64 exec, -1\n\t" \
    "v_exp_f32 v56, v56\n\t" \
    "v_exp_f32 v57, v57\n\t" \
    "v_exp_f32 v58, v58\n\t" \
    "v_mfma_f32_32x32x16_bf16 v[16:31], v[112:115], v[90:93], v[16:31]\n\t" \
    "v_exp_f32 v59, v59\n\t" \
    "v_cvt_pk_bf16_f32 v94, v56, v57\n\t" \
    "v_cvt_pk_bf16_f32 v95, v58, v59\n\t" \
    "v_exp_f32 v60, v60\n\t" \
    "v_exp_f32 v61, v61\n\t" \
    "v_exp_f32 v62, v62\n\t" \
    "v_exp_f32 v63, v63\n\t" \
    "v_cvt_pk_bf16_f32 v96, v60, v61\n\t" \
    "v_cvt_pk_bf16_f32 v97, v62, v63\n\t" \
    "v_mfma_f32_32x32x8_bf16 v[48:63], v[80:81], v[74:75], 0\n\t" \
    "ds_read_b64 v[110:111], v99 offset:7488\n\t" \
    "ds_read_b128 v[106:109], v98 offset:7488\n\t" \
    "v_exp_f32 v32, v32\n\t" \
    "v_exp_f32 v33, v33\n\t" \
    "v_exp_f32 v34, v34\n\t" \
    "v_exp_f32 v35, v35\n\t" \
    "v_cvt_pk_bf16_f32 v90, v32, v33\n\t" \
    "v_cvt_pk_bf16_f32 v91, v34, v35\n\t" \
    "v_mfma_f32_32x32x16_bf16 v[16:31], v[86:89], v[94:97], v[16:31]\n\t" \
    "ds_read_b128 v[86:89], v100 offset:5600\n\t" \
    "ds_read_b128 v[112:115], v100 offset:7488\n\t" \
    "v_exp_f32 v36, v36\n\t" \
    "v_exp_f32 v37, v37\n\t" \
    "v_exp_f32 v38, v38\n\t" \
    "v_exp_f32 v39, v39\n\t" \
    "v_cvt_pk_bf16_f32 v92, v36, v37\n\t" \
    "v_cvt_pk_bf16_f32 v93, v38, v39\n\t" \
    "v_mfma_f32_32x32x16_bf16 v[48:63], v[76:79], v[70:73], v[48:63]\n\t" \
    "v_exp_f32 v40, v40\n\t" \
    "v_exp_f32 v41, v41\n\t" \
    "v_exp_f32 v42, v42\n\t" \
    "v_mfma_f32_32x32x16_bf16 v[0:15], v[82:85], v[90:93], v[0:15]\n\t" \
    "v_exp_f32 v43, v43\n\t" \
    "v_cvt_pk_bf16_f32 v94, v40, v41\n\t" \
    "v_cvt_pk_bf16_f32 v95, v42, v43\n\t" \
    "v_exp_f32 v44, v44\n\t" \
    "v_exp_f32 v45, v45\n\t" \
    "v_exp_f32 v46, v46\n\t" \
    "v_exp_f32 v47, v47\n\t" \
    "v_cvt_pk_bf16_f32 v96, v44, v45\n\t" \
    "v_cvt_pk_bf16_f32 v97, v46, v47\n\t" \
    "s_waitcnt lgkmcnt(0)\n\t" \
    "v_mfma_f32_32x32x8_bf16 v[32:47], v[110:111], v[68:69], 0\n\t" \
    "v_exp_f32 v48, v48\n\t" \
    "v_exp_f32 v49, v49\n\t" \
    "v_exp_f32 v50, v50\n\t" \
    "v_exp_f32 v51, v51\n\t" \
    "v_cvt_pk_bf16_f32 v90, v48, v49\n\t" \
    "v_cvt_pk_bf16_f32 v91, v50, v51\n\t" \
    "v_mfma_f32_32x32x16_bf16 v[0:15], v[86:89], v[94:97], v[0:15]\n\t" \
    "v_exp_f32 v52, v52\n\t" \
    "v_exp_f32 v53, v53\n\t" \
    "v_exp_f32 v54, v54\n\t" \
    "v_exp_f32 v55, v55\n\t" \
    "v_cvt_pk_bf16_f32 v92, v52, v53\n\t" \
    "v_cvt_pk_bf16_f32 v93, v54, v55\n\t" \
    "v_mfma_f32_32x32x16_bf16 v[32:47], v[106:109], v[64:67], v[32:47]\n\t" \
    "s_min_u32 s94, %[wrow], 281\n\t" \
    "s_mul_i32 s95, s94, 144\n\t" \
    "s_add_u32 m0, s95, %[wlds]\n\t" \
    "s_mul_i32 s94, s94, 1152\n\t" \
    "s_add_u32 s96, %[wsl], s94\n\t" \
    "s_addc_u32 s97, %[wsh], 0\n\t" \
    "s_add_u32 %[wrow], %[wrow], 28\n\t" \
    "s_bitset0_b64 exec, 63\n\t" \
    "global_load_lds_dwordx4 v104, s[96:97]\n\t" \
    "s_mov_b64 exec, -1\n\t" \
    "v_exp_f32 v56, v56\n\t" \
    "v_exp_f32 v57, v57\n\t" \
    "v_exp_f32 v58, v58\n\t" \
    "v_mfma_f32_32x32x16_bf16 v[16:31], v[82:85], v[90:93], v[16:31]\n\t" \
    "v_exp_f32 v59, v59\n\t" \
    "v_cvt_pk_bf16_f32 v94, v56, v57\n\t" \
    "v_cvt_pk_bf16_f32 v95, v58, v59\n\t" \
    "v_exp_f32 v60, v60\n\t" \
    "v_exp_f32 v61, v61\n\t" \
    "v_exp_f32 v62, v62\n\t" \
    "v_exp_f32 v63, v63\n\t" \
    "v_cvt_pk_bf16_f32 v96, v60, v61\n\t" \
    "v_cvt_pk_bf16_f32 v97, v62, v63\n\t" \
    "v_mfma_f32_32x32x8_bf16 v[48:63], v[110:111], v[74:75], 0\n\t" \
    "ds_read_b64 v[80:81], v99 offset:9984\n\t" \
    "ds_read_b128 v[76:79], v98 offset:9984\n\t" \
    "v_exp_f32 v32, v32\n\t" \
    "v_exp_f32 v33, v33\n\t" \
    "v_exp_f32 v34, v34\n\t" \
    "v_exp_f32 v35, v35\n\t" \
    "v_cvt_pk_bf16_f32 v90, v32, v33\n\t" \
    "v_cvt_pk_bf16_f32 v91, v34, v35\n\t" \
    "v_mfma_f32_32x32x16_bf16 v[16:31], v[86:89], v[94:97], v[16:31]\n\t" \
    "ds_read_b128 v[86:89], v100 offset:8096\n\t" \
    "ds_read_b128 v[82:85], v100 offset:9984\n\t" \
    "v_exp_f32 v36, v36\n\t" \
    "v_exp_f32 v37, v37\n\t" \
    "v_exp_f32 v38, v38\n\t" \
    "v_exp_f32 v39, v39\n\t" \
    "v_cvt_pk_bf16_f32 v92, v36, v37\n\t" \
    "v_cvt_pk_bf16_f32 v93, v38, v39\n\t" \
    "v_mfma_f32_32x32x16_bf16 v[48:63], v[106:109], v[70:73], v[48:63]\n\t" \
    "v_exp_f32 v40, v40\n\t" \
    "v_exp_f32 v41, v41\n\t" \
    "v_exp_f32 v42, v42\n\t" \
    "v_mfma_f32_32x32x16_bf16 v[0:15], v[112:115], v[90:93], v[0:15]\n\t" \
    "v_exp_f32 v43, v43\n\t" \
    "v_cvt_pk_bf16_f32 v94, v40, v41\n\t" \
    "v_cvt_pk_bf16_f32 v95, v42, v43\n\t" \
    "v_exp_f32 v44, v44\n\t" \
    "v_exp_f32 v45, v45\n\t" \
    "v_exp_f32 v46, v46\n\t" \
    "v_exp_f32 v47, v47\n\t" \
    "v_cvt_pk_bf16_f32 v96, v44, v45\n\t" \
    "v_cvt_pk_bf16_f32 v97, v46, v47\n\t" \
    "s_waitcnt lgkmcnt(0)\n\t" \
    "v_mfma_f32_32x32x8_bf16 v[32:47], v[80:81], v[68:69], 0\n\t" \
    "v_exp_f32 v48, v48\n\t" \
    "v_exp_f32 v49, v49\n\t" \
    "v_exp_f32 v50, v50\n\t" \
    "v_exp_f32 v51, v51\n\t" \
    "v_cvt_pk_bf16_f32 v90, v48, v49\n\t" \
    "v_cvt_pk_bf16_f32 v91, v50, v51\n\t" \
    "v_mfma_f32_32x32x16_bf16 v[0:15], v[86:89], v[94:97], v[0:15]\n\t" \
    "v_exp_f32 v52, v52\n\t" \
    "v_exp_f32 v53, v53\n\t" \
    "v_exp_f32 v54, v54\n\t" \
    "v_exp_f32 v55, v55\n\t" \
    "v_cvt_pk_bf16_f32 v92, v52, v53\n\t" \
    "v_cvt_pk_bf16_f32 v93, v54, v55\n\t" \
    "v_mfma_f32_32x32x16_bf16 v[32:47], v[76:79], v[64:67], v[32:47]\n\t" \
    "s_min_u32 s94, %[wrow], 281\n\t" \
    "s_mul_i32 s95, s94, 144\n\t" \
    "s_add_u32 m0, s95, %[wlds]\n\t" \
    "s_mul_i32 s94, s94, 1152\n\t" \
    "s_add_u32 s96, %[wsl], s94\n\t" \
    "s_addc_u32 s97, %[wsh], 0\n\t" \
    "s_add_u32 %[wrow], %[wrow], 28\n\t" \
    "s_bitset0_b64 exec, 63\n\t" \
    "global_load_lds_dwordx4 v104, s[96:97]\n\t" \
    "s_mov_b64 exec, -1\n\t" \
    "v_exp_f32 v56, v56\n\t" \
    "v_exp_f32 v57, v57\n\t" \
    "v_exp_f32 v58, v58\n\t" \
    "v_mfma_f32_32x32x16_bf16 v[16:31], v[112:115], v[90:93], v[16:31]\n\t" \
    "v_exp_f32 v59, v59\n\t" \
    "v_cvt_pk_bf16_f32 v94, v56, v57\n\t" \
    "v_cvt_pk_bf16_f32 v95, v58, v59\n\t" \
    "v_exp_f32 v60, v60\n\t" \
    "v_exp_f32 v61, v61\n\t" \
    "v_exp_f32 v62, v62\n\t" \
    "v_exp_f32 v63, v63\n\t" \
    "v_cvt_pk_bf16_f32 v96, v60, v61\n\t" \
    "v_cvt_pk_bf16_f32 v97, v62, v63\n\t" \
    "v_add_u32 v98, 9984, v98\n\t" \
    "v_add_u32 v99, 9984, v99\n\t" \
    "v_add_u32 v100, 9984, v100\n\t" \
    "s_sub_u32 %[cnt], %[cnt], 1\n\t" \
    "s_cmp_lg_u32 %[cnt], 0\n\t" \
    "s_cbranch_scc1 3b\n\t" \
    "s_barrier\n\t" \
    "v_mfma_f32_32x32x8_bf16 v[48:63], v[80:81], v[74:75], 0\n\t" \
    "ds_read_b64 v[110:111], v99 offset:2496\n\t" \
    "ds_read_b128 v[106:109], v98 offset:2496\n\t" \
    "v_exp_f32 v32, v32\n\t" \
    "v_exp_f32 v33, v33\n\t" \
    "v_exp_f32 v34, v34\n\t" \
    "v_exp_f32 v35, v35\n\t" \
    "v_cvt_pk_bf16_f32 v90, v32, v33\n\t" \
    "v_cvt_pk_bf16_f32 v91, v34, v35\n\t" \
    "v_mfma_f32_32x32x16_bf16 v[16:31], v[86:89], v[94:97], v[16:31]\n\t" \
    "ds_read_b128 v[86:89], v100 offset:608\n\t" \
    "ds_read_b128 v[112:115], v100 offset:2496\n\t" \
    "v_exp_f32 v36, v36\n\t" \
    "v_exp_f32 v37, v37\n\t" \
    "v_exp_f32 v38, v38\n\t" \
    "v_exp_f32 v39, v39\n\t" \
    "v_cvt_pk_bf16_f32 v92, v36, v37\n\t" \
    "v_cvt_pk_bf16_f32 v93, v38, v39\n\t" \
    "v_mfma_f32_32x32x16_bf16 v[48:63], v[76:79], v[70:73], v[48:63]\n\t" \
    "v_exp_f32 v40, v40\n\t" \
    "v_exp_f32 v41, v41\n\t" \
    "v_exp_f32 v42, v42\n\t" \
    "v_mfma_f32_32x32x16_bf16 v[0:15], v[82:85], v[90:93], v[0:15]\n\t" \
    "v_exp_f32 v43, v43\n\t" \
    "v_cvt_pk_bf16_f32 v94, v40, v41\n\t" \
    "v_cvt_pk_bf16_f32 v95, v42, v43\n\t" \
    "v_exp_f32 v44, v44\n\t" \
    "v_exp_f32 v45, v45\n\t" \
    "v_exp_f32 v46, v46\n\t" \
    "v_exp_f32 v47, v47\n\t" \
    "v_cvt_pk_bf16_f32 v96, v44, v45\n\t" \
    "v_cvt_pk_bf16_f32 v97, v46, v47\n\t" \
    "s_waitcnt lgkmcnt(0)\n\t" \
    "v_mfma_f32_32x32x8_bf16 v[32:47], v[110:111], v[68:69], 0\n\t" \
    "v_exp_f32 v48, v48\n\t" \
    "v_exp_f32 v49, v49\n\t" \
    "v_exp_f32 v50, v50\n\t" \
    "v_exp_f32 v51, v51\n\t" \
    "v_cvt_pk_bf16_f32 v90, v48, v49\n\t" \
    "v_cvt_pk_bf16_f32 v91, v50, v51\n\t" \
    "v_mfma_f32_32x32x16_bf16 v[0:15], v[86:89], v[94:97], v[0:15]\n\t" \
    "v_exp_f32 v52, v52\n\t" \
    "v_exp_f32 v53, v53\n\t" \
    "v_exp_f32 v54, v54\n\t" \
    "v_exp_f32 v55, v55\n\t" \
    "v_cvt_pk_bf16_f32 v92, v52, v53\n\t" \
    "v_cvt_pk_bf16_f32 v93, v54, v55\n\t" \
    "v_mfma_f32_32x32x16_bf16 v[32:47], v[106:109], v[64:67], v[32:47]\n\t" \
    "s_min_u32 s94, %[wrow], 281\n\t" \
    "s_mul_i32 s95, s94, 144\n\t" \
    "s_add_u32 m0, s95, %[wlds]\n\t" \
    "s_mul_i32 s94, s94, 1152\n\t" \
    "s_add_u32 s96, %[wsl], s94\n\t" \
    "s_addc_u32 s97, %[wsh], 0\n\t" \
    "s_add_u32 %[wrow], %[wrow], 28\n\t" \
    "s_bitset0_b64 exec, 63\n\t" \
    "global_load_lds_dwordx4 v104, s[96:97]\n\t" \
    "s_mov_b64 exec, -1\n\t" \
    "v_exp_f32 v56, v56\n\t" \
    "v_exp_f32 v57, v57\n\t" \
    "v_exp_f32 v58, v58\n\t" \
    "v_mfma_f32_32x32x16_bf16 v[16:31], v[82:85], v[90:93], v[16:31]\n\t" \
    "v_exp_f32 v59, v59\n\t" \
    "v_cvt_pk_bf16_f32 v94, v56, v57\n\t" \
    "v_cvt_pk_bf16_f32 v95, v58, v59\n\t" \
    "v_exp_f32 v60, v60\n\t" \
    "v_exp_f32 v61, v61\n\t" \
    "v_exp_f32 v62, v62\n\t" \
    "v_exp_f32 v63, v63\n\t" \
    "v_cvt_pk_bf16_f32 v96, v60, v61\n\t" \
    "v_cvt_pk_bf16_f32 v97, v62, v63\n\t" \
    "v_mfma_f32_32x32x8_bf16 v[48:63], v[110:111], v[74:75], 0\n\t" \
    "ds_read_b64 v[80:81], v99 offset:4992\n\t" \
    "ds_read_b128 v[76:79], v98 offset:4992\n\t" \
    "v_exp_f32 v32, v32\n\t" \
    "v_exp_f32 v33, v33\n\t" \
    "v_exp_f32 v34, v34\n\t" \
    "v_exp_f32 v35, v35\n\t" \
    "v_cvt_pk_bf16_f32 v90, v32, v33\n\t" \
    "v_cvt_pk_bf16_f32 v91, v34, v35\n\t" \
    "v_mfma_f32_32x32x16_bf16 v[16:31], v[86:89], v[94:97], v[16:31]\n\t" \
    "ds_read_b128 v[86:89], v100 offset:3104\n\t" \
    "ds_read_b128 v[82:85], v100 offset:4992\n\t" \
    "v_exp_f32 v36, v36\n\t" \
    "v_exp_f32 v37, v37\n\t" \
    "v_exp_f32 v38, v38\n\t" \
    "v_exp_f32 v39, v39\n\t" \
    "v_cvt_pk_bf16_f32 v92, v36, v37\n\t" \
    "v_cvt_pk_bf16_f32 v93, v38, v39\n\t" \
    "v_mfma_f32_32x32x16_bf16 v[48:63], v[106:109], v[70:73], v[48:63]\n\t" \
    "v_exp_f32 v40, v40\n\t" \
    "v_exp_f32 v41, v41\n\t" \
    "v_exp_f32 v42, v42\n\t" \
    "v_mfma_f32_32x32x16_bf16 v[0:15], v[112:115], v[90:93], v[0:15]\n\t" \
    "v_exp_f32 v43, v43\n\t" \
    "v_cvt_pk_bf16_f32 v94, v40, v41\n\t" \
    "v_cvt_pk_bf16_f32 v95, v42, v43\n\t" \
    "v_exp_f32 v44, v44\n\t" \
    "v_exp_f32 v45, v45\n\t" \
    "v_exp_f32 v46, v46\n\t" \
    "v_exp_f32 v47, v47\n\t" \
    "v_cvt_pk_bf16_f32 v96, v44, v45\n\t" \
    "v_cvt_pk_bf16_f32 v97, v46, v47\n\t" \
    "s_waitcnt lgkmcnt(0)\n\t" \
    "v_mfma_f32_32x32x8_bf16 v[32:47], v[80:81], v[68:69], 0\n\t" \
    "v_exp_f32 v48, v48\n\t" \
    "v_exp_f32 v49, v49\n\t" \
    "v_exp_f32 v50, v50\n\t" \
    "v_exp_f32 v51, v51\n\t" \
    "v_cvt_pk_bf16_f32 v90, v48, v49\n\t" \
    "v_cvt_pk_bf16_f32 v91, v50, v51\n\t" \
    "v_mfma_f32_32x32x16_bf16 v[0:15], v[86:89], v[94:97], v[0:15]\n\t" \
    "v_exp_f32 v52, v52\n\t" \
    "v_exp_f32 v53, v53\n\t" \
    "v_exp_f32 v54, v54\n\t" \
    "v_exp_f32 v55, v55\n\t" \
    "v_cvt_pk_bf16_f32 v92, v52, v53\n\t" \
    "v_cvt_pk_bf16_f32 v93, v54, v55\n\t" \
    "v_mfma_f32_32x32x16_bf16 v[32:47], v[76:79], v[64:67], v[32:47]\n\t" \
    "s_min_u32 s94, %[wrow], 281\n\t" \
    "s_mul_i32 s95, s94, 144\n\t" \
    "s_add_u32 m0, s95, %[wlds]\n\t" \
    "s_mul_i32 s94, s94, 1152\n\t" \
    "s_add_u32 s96, %[wsl], s94\n\t" \
    "s_addc_u32 s97, %[wsh], 0\n\t" \
    "s_add_u32 %[wrow], %[wrow], 28\n\t" \
    "s_bitset0_b64 exec, 63\n\t" \
    "global_load_lds_dwordx4 v104, s[96:97]\n\t" \
    "s_mov_b64 exec, -1\n\t" \
    "v_exp_f32 v56, v56\n\t" \
    "v_exp_f32 v57, v57\n\t" \
    "v_exp_f32 v58, v58\n\t" \
    "v_mfma_f32_32x32x16_bf16 v[16:31], v[112:115], v[90:93], v[16:31]\n\t" \
    "v_exp_f32 v59, v59\n\t" \
    "v_cvt_pk_bf16_f32 v94, v56, v57\n\t" \
    "v_cvt_pk_bf16_f32 v95, v58, v59\n\t" \
    "v_exp_f32 v60, v60\n\t" \
    "v_exp_f32 v61, v61\n\t" \
    "v_exp_f32 v62, v62\n\t" \
    "v_exp_f32 v63, v63\n\t" \
    "v_cvt_pk_bf16_f32 v96, v60, v61\n\t" \
    "v_cvt_pk_bf16_f32 v97, v62, v63\n\t" \
    "v_mfma_f32_32x32x8_bf16 v[48:63], v[80:81], v[74:75], 0\n\t" \
    "ds_read_b64 v[110:111], v99 offset:7488\n\t" \
    "ds_read_b128 v[106:109], v98 offset:7488\n\t" \
    "v_exp_f32 v32, v32\n\t" \
    "v_exp_f32 v33, v33\n\t" \
    "v_exp_f32 v34, v34\n\t" \
    "v_exp_f32 v35, v35\n\t" \
    "v_cvt_pk_bf16_f32 v90, v32, v33\n\t" \
    "v_cvt_pk_bf16_f32 v91, v34, v35\n\t" \
    "v_mfma_f32_32x32x16_bf16 v[16:31], v[86:89], v[94:97], v[16:31]\n\t" \
    "ds_read_b128 v[86:89], v100 offset:5600\n\t" \
    "ds_read_b128 v[112:115], v100 offset:7488\n\t" \
    "v_exp_f32 v36, v36\n\t" \
    "v_exp_f32 v37, v37\n\t" \
    "v_exp_f32 v38, v38\n\t" \
    "v_exp_f32 v39, v39\n\t" \
    "v_cvt_pk_bf16_f32 v92, v36, v37\n\t" \
    "v_cvt_pk_bf16_f32 v93, v38, v39\n\t" \
    "v_mfma_f32_32x32x16_bf16 v[48:63], v[76:79], v[70:73], v[48:63]\n\t" \
    "v_exp_f32 v40, v40\n\t" \
    "v_exp_f32 v41, v41\n\t" \
    "v_exp_f32 v42, v42\n\t" \
    "v_mfma_f32_32x32x16_bf16 v[0:15], v[82:85], v[90:93], v[0:15]\n\t" \
    "v_exp_f32 v43, v43\n\t" \
    "v_cvt_pk_bf16_f32 v94, v40, v41\n\t" \
    "v_cvt_pk_bf16_f32 v95, v42, v43\n\t" \
    "v_exp_f32 v44, v44\n\t" \
    "v_exp_f32 v45, v45\n\t" \
    "v_exp_f32 v46, v46\n\t" \
    "v_exp_f32 v47, v47\n\t" \
    "v_cvt_pk_bf16_f32 v96, v44, v45\n\t" \
    "v_cvt_pk_bf16_f32 v97, v46, v47\n\t" \
    "s_waitcnt lgkmcnt(0)\n\t" \
    "v_mfma_f32_32x32x8_bf16 v[32:47], v[110:111], v[68:69], 0\n\t" \
    "v_exp_f32 v48, v48\n\t" \
    "v_exp_f32 v49, v49\n\t" \
    "v_exp_f32 v50, v50\n\t" \
    "v_exp_f32 v51, v51\n\t" \
    "v_cvt_pk_bf16_f32 v90, v48, v49\n\t" \
    "v_cvt_pk_bf16_f32 v91, v50, v51\n\t" \
    "v_mfma_f32_32x32x16_bf16 v[0:15], v[86:89], v[94:97], v[0:15]\n\t" \
    "v_exp_f32 v52, v52\n\t" \
    "v_exp_f32 v53, v53\n\t" \
    "v_exp_f32 v54, v54\n\t" \
    "v_exp_f32 v55, v55\n\t" \
    "v_cvt_pk_bf16_f32 v92, v52, v53\n\t" \
    "v_cvt_pk_bf16_f32 v93, v54, v55\n\t" \
    "v_mfma_f32_32x32x16_bf16 v[32:47], v[106:109], v[64:67], v[32:47]\n\t" \
    "v_exp_f32 v56, v56\n\t" \
    "v_exp_f32 v57, v57\n\t" \
    "v_exp_f32 v58, v58\n\t" \
    "v_mfma_f32_32x32x16_bf16 v[16:31], v[82:85], v[90:93], v[16:31]\n\t" \
    "v_exp_f32 v59, v59\n\t" \
    "v_cvt_pk_bf16_f32 v94, v56, v57\n\t" \
    "v_cvt_pk_bf16_f32 v95, v58, v59\n\t" \
    "v_exp_f32 v60, v60\n\t" \
    "v_exp_f32 v61, v61\n\t" \
    "v_exp_f32 v62, v62\n\t" \
    "v_exp_f32 v63, v63\n\t" \
    "v_cvt_pk_bf16_f32 v96, v60, v61\n\t" \
    "v_cvt_pk_bf16_f32 v97, v62, v63\n\t" \
    "v_mfma_f32_32x32x8_bf16 v[48:63], v[110:111], v[74:75], 0\n\t" \
    "v_exp_f32 v32, v32\n\t" \
    "v_exp_f32 v33, v33\n\t" \
    "v_exp_f32 v34, v34\n\t" \
    "v_exp_f32 v35, v35\n\t" \
    "v_cvt_pk_bf16_f32 v90, v32, v33\n\t" \
    "v_cvt_pk_bf16_f32 v91, v34, v35\n\t" \
    "v_mfma_f32_32x32x16_bf16 v[16:31], v[86:89], v[94:97], v[16:31]\n\t" \
    "ds_read_b128 v[86:89], v100 offset:8096\n\t" \
    "v_exp_f32 v36, v36\n\t" \
    "v_exp_f32 v37, v37\n\t" \
    "v_exp_f32 v38, v38\n\t" \
    "v_exp_f32 v39, v39\n\t" \
    "v_cvt_pk_bf16_f32 v92, v36, v37\n\t" \
    "v_cvt_pk_bf16_f32 v93, v38, v39\n\t" \
    "v_mfma_f32_32x32x16_bf16 v[48:63], v[106:109], v[70:73], v[48:63]\n\t" \
    "v_exp_f32 v40, v40\n\t" \
    "v_exp_f32 v41, v41\n\t" \
    "v_exp_f32 v42, v42\n\t" \
    "v_mfma_f32_32x32x16_bf16 v[0:15], v[112:115], v[90:93], v[0:15]\n\t" \
    "v_exp_f32 v43, v43\n\t" \
    "v_cvt_pk_bf16_f32 v94, v40, v41\n\t" \
    "v_cvt_pk_bf16_f32 v95, v42, v43\n\t" \
    "v_exp_f32 v44, v44\n\t" \
    "v_exp_f32 v45, v45\n\t" \
    "v_exp_f32 v46, v46\n\t" \
    "v_exp_f32 v47, v47\n\t" \
    "v_cvt_pk_bf16_f32 v96, v44, v45\n\t" \
    "v_cvt_pk_bf16_f32 v97, v46, v47\n\t" \
    "s_waitcnt lgkmcnt(0)\n\t" \
    "v_exp_f32 v48, v48\n\t" \
    "v_exp_f32 v49, v49\n\t" \
    "v_exp_f32 v50, v50\n\t" \
    "v_exp_f32 v51, v51\n\t" \
    "v_cvt_pk_bf16_f32 v90, v48, v49\n\t" \
    "v_cvt_pk_bf16_f32 v91, v50, v51\n\t" \
    "v_mfma_f32_32x32x16_bf16 v[0:15], v[86:89], v[94:97], v[0:15]\n\t" \
    "v_exp_f32 v52, v52\n\t" \
    "v_exp_f32 v53, v53\n\t" \
    "v_exp_f32 v54, v54\n\t" \
    "v_exp_f32 v55, v55\n\t" \
    "v_cvt_pk_bf16_f32 v92, v52, v53\n\t" \
    "v_cvt_pk_bf16_f32 v93, v54, v55\n\t" \
    "v_exp_f32 v56, v56\n\t" \
    "v_exp_f32 v57, v57\n\t" \
    "v_exp_f32 v58, v58\n\t" \
    "v_mfma_f32_32x32x16_bf16 v[16:31], v[112:115], v[90:93], v[16:31]\n\t" \
    "v_exp_f32 v59, v59\n\t" \
    "v_cvt_pk_bf16_f32 v94, v56, v57\n\t" \
    "v_cvt_pk_bf16_f32 v95, v58, v59\n\t" \
    "v_exp_f32 v60, v60\n\t" \
    "v_exp_f32 v61, v61\n\t" \
    "v_exp_f32 v62, v62\n\t" \
    "v_exp_f32 v63, v63\n\t" \
    "v_cvt_pk_bf16_f32 v96, v60, v61\n\t" \
    "v_cvt_pk_bf16_f32 v97, v62, v63\n\t" \
    "s_nop 1\n\t" \
    "v_mfma_f32_32x32x16_bf16 v[16:31], v[86:89], v[94:97], v[16:31]\n\t" \
    "s_nop 15\n\t" \
    ""
constexpr int CH_B = 8 * KV_T;
constexpr int K2_LDS = 163840;
constexpr int YT_ROW = 4096;
constexpr int OT_B = 16 * 1024;
constexpr int OTAIL = 128 * OT_B;
#define ATT_REGS_Q "{v[64:67]}"(qam), "{v[68:69]}"(qat), "{v[70:73]}"(qbm), "{v[74:75]}"(qbt)

__device__ __forceinline__ void poll32(unsigned* flags, int lane, unsigned tag) {
    for (unsigned spins = 0; spins < (1u << 17); spins++) {
        const unsigned v = __hip_atomic_load(flags + (lane & 31), __ATOMIC_RELAXED, __HIP_MEMORY_SCOPE_AGENT);
        if (__all(v == tag)) break;
        __builtin_amdgcn_s_sleep(1);
    }
}

__device__ __forceinline__ int ka_off(int w) { return w < 4 ? 19968 + w * KG_LDS : 99840 + (w - 4) * KG_LDS; }
constexpr int UT_OFF = 128672, S2_OFF = 142496;
__device__ __forceinline__ int slot_off(int w) { return ((w >> 1) == 3 ? S2_OFF : ka_off(2 * (w >> 1) + 1)) + (w & 1) * 6144; }

__device__ __forceinline__ f32x16 attn_safe(const char* __restrict__ vimg, int head, int lane, u32x4 qm, u32x2 qt_) {
    const int r32 = lane & 31, hh = lane >> 5;
    f32x16 o; for (int i = 0; i < 16; i++) o[i] = 0.f;
    float m = -3.0e38f;
    const int vr = r32 < 19 ? r32 : 18;
    #pragma unroll 1
    for (int t = 0; t < 64; t++) {
        const char* tp = vimg + (size_t)head * KV_H + t * KV_T;
        const u32x4 kmn = *(const u32x4*)(tp + lane * 16);
        const u32x2 ktl = *(const u32x2*)(tp + 1024 + r32 * 8);
        const char* vp = tp + 1280 + (hh * 19 + vr) * 16;
        const bf16x8 va = *(const bf16x8*)vp, vb = *(const bf16x8*)(vp + 608);
        f32x16 s; for (int i = 0; i < 16; i++) s[i] = 0.f;
        s = __builtin_amdgcn_mfma_f32_32x32x8bf16_1k(__builtin_bit_cast(bf16x4, ktl), __builtin_bit_cast(bf16x4, qt_), s, 0, 0, 0);
        s = MFMA32(__builtin_bit_cast(bf16x8, kmn), __builtin_bit_cast(bf16x8, qm), s);
        float mx = s[0];
        for (int i = 1; i < 16; i++) mx = fmaxf(mx, s[i]);
        mx = fmaxf(mx, __shfl_xor(mx, 32));
        const float mn = fmaxf(m, mx), al = __builtin_amdgcn_exp2f(m - mn);
        m = mn;
        for (int i = 0; i < 16; i++) o[i] *= al;
        u32x4 p0, p1;
        for (int i = 0; i < 4; i++) { p0[i] = pk(__builtin_amdgcn_exp2f(s[2 * i] - m), __builtin_amdgcn_exp2f(s[2 * i + 1] - m));
                                      p1[i] = pk(__builtin_amdgcn_exp2f(s[8 + 2 * i] - m), __builtin_amdgcn_exp2f(s[8 + 2 * i + 1] - m)); }
        o = MFMA32(va, __builtin_bit_cast(bf16x8, p0), o);
        o = MFMA32(vb, __builtin_bit_cast(bf16x8, p1), o);
    }
    return o;
}

__device__ __forceinline__ bool tile_unsafe(const f32x16& o, float l, int lane) {
    bool bad = !(l > 8.6736174e-19f && l < 1.1529215e18f);
    #pragma unroll
    for (int i = 0; i < 10; i++) bad |= !(__builtin_fabsf(o[i]) < 3.0e38f) && (i < 8 || lane < 32);
    return __any(bad);
}
__device__ __forceinline__ void publish_tile(char* __restrict__ oimg, const f32x16& o, float l, int head, int qtile, int lane) {
    const int r32 = lane & 31, hh = lane >> 5;
    const float inv = __builtin_amdgcn_rcpf(l);
    const int q = qtile * 32 + r32;
    st16_sc1(oimg, (unsigned)(q >> 4) * OT_B + (head >> 1) * 1024 + ((q & 15) + 16 * (2 * (head & 1) + hh)) * 16,
             (u32x4){pk(o[0] * inv, o[1] * inv), pk(o[2] * inv, o[3] * inv), pk(o[4] * inv, o[5] * inv), pk(o[6] * inv, o[7] * inv)});
    if (lane < 32) st4_sc1(oimg, OTAIL + head * 8192 + q * 4, pk(o[8] * inv, o[9] * inv));
}
__device__ __forceinline__ void finish_tiles(const char* __restrict__ vimg, char* __restrict__ oimg, f32x16 oa, f32x16 ob, LDS char* red, int head, int qt, int lane,
                                             u32x4 qam, u32x2 qat, u32x4 qbm, u32x2 qbt) {
    const int r32 = lane & 31;
    #pragma unroll
    for (int i = 0; i < 3; i++) {
        const f32x4 pa = *(LDS f32x4*)(red + i * 1024), pb = *(LDS f32x4*)(red + 3072 + i * 1024);
        #pragma unroll
        for (int e = 0; e < 4; e++) { oa[4 * i + e] += pa[e]; ob[4 * i + e] += pb[e]; }
    }
    float la = __shfl(oa[10], r32), lb = __shfl(ob[10], r32);
    const bool ba = tile_unsafe(oa, la, lane), bb = tile_unsafe(ob, lb, lane);
    if (ba) { oa = attn_safe(vimg, head, lane, qam, qat); la = __shfl(oa[10], r32); }
    if (bb) { ob = attn_safe(vimg, head, lane, qbm, qbt); lb = __shfl(ob[10], r32); }
    publish_tile(oimg, oa, la, head, qt, lane);
    publish_tile(oimg, ob, lb, head, qt + 1, lane);
}

template<int KH>
__device__ __forceinline__ void oproj_half(f32x4 (&acc)[5], const char* __restrict__ oimg, unsigned go, unsigned gt, LDS char* wa, LDS char* wt) {
    u32x4 of[9];
    unsigned t0_, t1_, t2_, t3_, t4_, t5_, t6_, t7_;
    if (KH == 0) {
        asm volatile("global_load_dwordx4 %0, %9, %11 sc1\n\tglobal_load_dwordx4 %1, %9, %11 offset:1024 sc1\n\tglobal_load_dwordx4 %2, %9, %11 offset:2048 sc1\n\tglobal_load_dwordx4 %3, %9, %11 offset:3072 sc1\n\t"
                     "global_load_dwordx4 %4, %10, %11 sc1\n\tglobal_load_dwordx4 %5, %10, %11 offset:1024 sc1\n\tglobal_load_dwordx4 %6, %10, %11 offset:2048 sc1\n\tglobal_load_dwordx4 %7, %10, %11 offset:3072 sc1\n\t"
                     "global_load_dwordx4 %8, %12, %11 sc1"
                     : "=&v"(of[0]), "=&v"(of[1]), "=&v"(of[2]), "=&v"(of[3]), "=&v"(of[4]), "=&v"(of[5]), "=&v"(of[6]), "=&v"(of[7]), "=&v"(of[8]) : "v"(go), "v"(go + 4096), "s"(oimg), "v"(go + 8192) : "memory");
    } else {
        asm volatile("global_load_dwordx4 %0, %15, %17 offset:1024 sc1\n\tglobal_load_dwordx4 %1, %15, %17 offset:2048 sc1\n\tglobal_load_dwordx4 %2, %15, %17 offset:3072 sc1\n\t"
                     "global_load_dwordx4 %3, %16, %17 sc1\n\tglobal_load_dwordx4 %4, %16, %17 offset:1024 sc1\n\tglobal_load_dwordx4 %5, %16, %17 offset:2048 sc1\n\tglobal_load_dwordx4 %6, %16, %17 offset:3072 sc1\n\t"
                     "global_load_dword %7, %18, %17 sc1\n\tglobal_load_dword %8, %19, %17 sc1\n\tglobal_load_dword %9, %20, %17 sc1\n\tglobal_load_dword %10, %21, %17 sc1\n\t"
                     "global_load_dword %11, %22, %17 sc1\n\tglobal_load_dword %12, %23, %17 sc1\n\tglobal_load_dword %13, %24, %17 sc1\n\tglobal_load_dword %14, %25, %17 sc1"
                     : "=&v"(of[0]), "=&v"(of[1]), "=&v"(of[2]), "=&v"(of[3]), "=&v"(of[4]), "=&v"(of[5]), "=&v"(of[6]),
                       "=&v"(t0_), "=&v"(t1_), "=&v"(t2_), "=&v"(t3_), "=&v"(t4_), "=&v"(t5_), "=&v"(t6_), "=&v"(t7_)
                     : "v"(go + 8192), "v"(go + 12288), "s"(oimg), "v"(gt), "v"(gt + 8192), "v"(gt + 16384), "v"(gt + 24576),
                       "v"(gt + 131072), "v"(gt + 139264), "v"(gt + 147456), "v"(gt + 155648) : "memory");
    }
    bf16x8 wf[2][5];
    #define OP_LOAD(B, KS) { LDS char* p1 = (KS) < 8 ? wa + (KS) * 5184 : (KS) < 16 ? wa + 79872 + ((KS) - 8) * 5184 : wt + ((KS) - 16) * 79872; \
        LDS char* p2 = p1 + ((KS) < 16 ? 1152 : 5184); \
        _Pragma("unroll") for (int j = 0; j < 5; j++) wf[B][j] = cat8(tr4(p1 + 32 * j), tr4(p2 + 32 * j)); }
    OP_LOAD(0, 9 * KH)
    #pragma unroll
    for (int i = 0; i < 9; i++) {
        if (i < 8) OP_LOAD((i + 1) & 1, 9 * KH + i + 1)
        if (KH == 0) asm volatile("s_waitcnt vmcnt(%1)" : "+v"(of[i]) : "i"(8 - i));
        else if (i < 7) asm volatile("s_waitcnt vmcnt(%1)" : "+v"(of[i]) : "i"(14 - i));
        else if (i == 7) {
            asm volatile("s_waitcnt vmcnt(0)" : "+v"(t0_), "+v"(t1_), "+v"(t2_), "+v"(t3_), "+v"(t4_), "+v"(t5_), "+v"(t6_), "+v"(t7_));
            of[7] = (u32x4){t0_, t1_, t2_, t3_}; of[8] = (u32x4){t4_, t5_, t6_, t7_};
        }
        __builtin_amdgcn_sched_barrier(0);
        #pragma unroll
        for (int j = 0; j < 5; j++) acc[j] = MFMA16(wf[i & 1][j], __builtin_bit_cast(bf16x8, of[i]), acc[j]);
        __builtin_amdgcn_sched_barrier(0);
    }
    #undef OP_LOAD
}

struct K2Args { const char* vimg; const char* ut; const char* kimg; const u16* Are; const u16* Aim; const u16* Bre; const u16* Bim; const u16* Cre; const u16* Cim; const u16* logdt;
                const u16* u; const u16* Dskip; const u16* wo; char* yt; char* oimg; unsigned* flags; u16* out; };

__global__ __launch_bounds__(512, 2) void k2_kernel(K2Args a) {
    extern __shared__ __attribute__((aligned(16))) char smem[];
    const int tid = threadIdx.x, lane = tid & 63;
    const int w = __builtin_amdgcn_readfirstlane(tid >> 6);
    const int b = blockIdx.x;
    const unsigned tag = 1u;
    const int head = (b & 7) * 4 + ((b >> 3) & 3), qb = b >> 5;
    const int qp_i = w & 3, kpart = w >> 2;
    const int qt = qb * 8 + qp_i * 2;
    const int r32 = lane & 31, hh = lane >> 5;
    const unsigned lds0 = (unsigned)(uintptr_t)(LDS char*)smem;
    LDS char* sm = (LDS char*)smem;
    {
        unsigned off = lane * 16 + w * (K2_SELF_BYTES / 8);
        asm volatile("s_getpc_b64 s[40:41]\n\tglobal_load_dwordx4 v[200:203], %0, s[40:41]\n\tglobal_load_dwordx4 v[200:203], %0, s[40:41] offset:1024\n\t"
                     "global_load_dwordx4 v[200:203], %0, s[40:41] offset:2048\n\tglobal_load_dwordx4 v[200:203], %0, s[40:41] offset:3072"
                     :: "v"(off) : "memory", "s40", "s41", "v200", "v201", "v202", "v203");
    }
    const int chA = 72 * (b >> 5) + ((2 * b) & 63), chS = 72 * (b >> 5) + 64 + ((b >> 2) & 7), shq = b & 3;
    const int area = w >> 1;
    KgPar kp = kgen_params(a.Are, a.Aim, a.Bre, a.Bim, a.Cre, a.Cim, a.logdt, chA + (area >> 1), area & 1, lane);
    u32x4 tbA = *(const u32x4*)(KEXP_TAB + ((w & 3) * 64 + lane) * 16);
    unsigned tbA8 = KEXP_TAB[((w & 3) * 64 + lane) * 16 + 8];
    asm volatile("" : "+v"(kp.a), "+v"(kp.rv), "+v"(kp.W.r), "+v"(kp.W.i), "+v"(tbA), "+v"(tbA8));
    if ((w & 1) == 0) kgen_Y(kp, sm + ka_off(area), lane); else kgen_X(kp, sm + ka_off(area), lane);
    asm volatile("s_waitcnt lgkmcnt(0)\n\ts_barrier" ::: "memory");
    if ((w & 1) == 0) kgen_contract(sm + ka_off(area), lane);
    else {
        const unsigned vd_ = lane * 16;
        if (w < 7) {
            const int ci = w >> 1;
            const char* ub = a.ut + (size_t)(ci == 2 ? chS : chA + ci) * UT_ROW;
            const unsigned ul = lds0 + UT_OFF + ci * UT_ROW;
            glds_x4(vd_, ub, ul);
            if (lane < 32) glds16s(vd_, ub + 4096, ul + 4096);
        } else {
            const char* kb = a.kimg + (size_t)(8 * (b >> 5) + ((b >> 2) & 7)) * KI_B;
            const unsigned kl = lds0 + ka_off(4) + 4096;
            glds_x4(vd_, kb, kl); glds_x4(vd_, kb + 4096, kl + 4096);
            if (lane < 8) glds16s(vd_, kb + 8192, kl + 8192);
        }
    }
    asm volatile("s_waitcnt lgkmcnt(0)\n\ts_barrier" ::: "memory");
    kgen_expand(sm + ka_off(2 * (w >> 2)) + 4096, sm + ka_off(2 * (w >> 2)), tbA, tbA8, w & 3, lane);
    asm volatile("s_waitcnt vmcnt(0) lgkmcnt(0)\n\ts_barrier" ::: "memory");
    u32x4 qam, qbm; u32x2 qat, qbt;
    {
        const char* qa = a.vimg + (size_t)(32 + head) * KV_H + qt * KV_T;
        qam = *(const u32x4*)(qa + lane * 16); qbm = *(const u32x4*)(qa + KV_T + lane * 16);
        qat = *(const u32x2*)(qa + 1024 + r32 * 8); qbt = *(const u32x2*)(qa + KV_T + 1024 + r32 * 8);
    }
    const int cb = b >> 5, tb = (b & 7) * 4 + ((b >> 3) & 3);
    u32x4 uv[3], dv0, yv[3];
    const int trow = tb * 64 + (w & 3) * 16, i2 = lane + 128 < 144 ? lane + 128 : lane;
    dv0 = *(const u32x4*)((const char*)a.Dskip + cb * 144 + (lane % 9) * 16);
    #pragma unroll
    for (int z = 0; z < 3; z++) { const int t = 7 * z + lane / 9; uv[z] = *(const u32x4*)((const char*)a.u + (size_t)(trow + (t < 16 ? t : 15)) * 1152 + cb * 144 + (lane % 9) * 16); }
    if (w >= 4) {
        const unsigned vd_ = lane * 16;
        const int q = w - 4;
        const unsigned o = (q >> 1) * 32 * KV_T + (q & 1) * 10240;
        const char* kvb = a.vimg + (size_t)head * KV_H + o;
        glds_x4(vd_, kvb, lds0 + o); glds_x4(vd_, kvb + 4096, lds0 + o + 4096);
        glds16s(vd_, kvb + 8192, lds0 + o + 8192);
        if ((q & 1) == 0 || lane < 32) glds16s(vd_, kvb + 9216, lds0 + o + 9216);
    }
    {
        f32x4 acc[8], accS[2];
        #pragma unroll
        for (int c = 0; c < 8; c++) acc[c] = (f32x4){0.f, 0.f, 0.f, 0.f};
        accS[0] = accS[1] = (f32x4){0.f, 0.f, 0.f, 0.f};
        const int ci = w >> 2;
        if (w < 4) conv_shared(accS, sm + ka_off(4) + 4096, sm + UT_OFF + 2 * UT_ROW, 2 * w, shq, lane);
        conv_units2(acc, sm + ka_off(2 * ci) + 4096, sm + UT_OFF + ci * UT_ROW, 2 * (w & 3), lane);
        if (hh || (head & 1)) { qat[0] = 0; qbt[0] = 0; }
        if (hh || !(head & 1)) { qat[1] = 0; qbt[1] = 0; }
        asm volatile("s_waitcnt vmcnt(0)" : "+v"(qam), "+v"(qbm), "+v"(qat), "+v"(qbt), "+v"(dv0), "+v"(uv[0]), "+v"(uv[1]), "+v"(uv[2]));
        const int j = w & 3;
        LDS char* sl = sm + slot_off(w) + lane * 16;
        #define CV_PUT(J) { _Pragma("unroll") for (int c = 0; c < 8; c++) if ((c >> 1) != (J)) *(LDS f32x4*)(sl + (c < 2 * (J) ? c : c - 2) * 1024) = acc[c]; y0 = acc[2 * (J)]; y1 = acc[2 * (J) + 1]; asm volatile("" ::: "memory"); }
        f32x4 y0, y1;
        if (j == 0) CV_PUT(0) else if (j == 1) CV_PUT(1) else if (j == 2) CV_PUT(2) else CV_PUT(3)
        #undef CV_PUT
        if (w < 4) { LDS char* ss = sm + slot_off(2 * w) - ((2 * w) & 1) * 6144 + 12288 + lane * 16; *(LDS f32x4*)ss = accS[0]; *(LDS f32x4*)(ss + 1024) = accS[1]; }
        asm volatile("s_waitcnt lgkmcnt(0)\n\ts_barrier" ::: "memory");
        #pragma unroll
        for (int k = 1; k < 4; k++) {
            const int jo = (j + k) & 3, wo_ = (w & 4) + jo;
            LDS char* p = sm + slot_off(wo_) + (j < jo ? 2 * j : 2 * j - 2) * 1024 + lane * 16;
            y0 += *(LDS f32x4*)p; y1 += *(LDS f32x4*)(p + 1024);
        }
        const unsigned tl = ((lane & 15) * 16 + (lane >> 4) * 4) * 2;
        const unsigned yo = (unsigned)(chA + ci) * YT_ROW + j * 1024 + tl;
        st8_sc1(a.yt, yo, (u32x2){pk(y0[0], y0[1]), pk(y0[2], y0[3])});
        st8_sc1(a.yt, yo + 512, (u32x2){pk(y1[0], y1[1]), pk(y1[2], y1[3])});
        if (w == 4 || w == 5) {
            const int jj = w - 4;
            f32x4 ys = *(LDS f32x4*)(sm + slot_off(0) + 12288 + jj * 1024 + lane * 16);
            #pragma unroll
            for (int k = 1; k < 4; k++) ys += *(LDS f32x4*)(sm + slot_off(2 * k) + 12288 + jj * 1024 + lane * 16);
            const int c = jj == 0 ? shq : 7 - shq;
            st8_sc1(a.yt, (unsigned)chS * YT_ROW + c * 512 + tl, (u32x2){pk(ys[0], ys[1]), pk(ys[2], ys[3])});
        }
        asm volatile("s_waitcnt lgkmcnt(0)\n\ts_barrier" ::: "memory");
    }
    const char* hb = a.vimg + (size_t)head * KV_H;
    unsigned so = kpart * 32 * KV_T + CH_B + (w & 3) * 1024;
    const unsigned vd = lane * 16;
    #pragma unroll
    for (int i = 0; i < 3; i++) glds16s(vd, hb + so + i * 4096, lds0 + so + i * 4096);
    so += 3 * 4096;
    const unsigned long long hbu = (unsigned long long)(uintptr_t)hb;
    const u32x4 srd = {(unsigned)__builtin_amdgcn_readfirstlane((unsigned)hbu), (unsigned)__builtin_amdgcn_readfirstlane((unsigned)(hbu >> 32)) & 0xffffu, (unsigned)(KV_H + 4096), 0x00020000u};
    const unsigned tb_ = lds0 + kpart * 32 * KV_T;
    unsigned ak = tb_ + lane * 16, at = tb_ + 1024 + r32 * 8, av = tb_ + 1280 + (hh * 19 + r32) * 16;
    if (r32 >= 19) av = 0x80000000u;
    if (lane >= 32) at = 0x80000000u;
    const char* wsrc = (const char*)a.wo + (size_t)kpart * 288 * 1152 + cb * 144;
    const unsigned wlds = lds0 + kpart * 79872, vw = (lane / 9) * 1152 + (lane % 9) * 16;
    int wrow = 7 * (w & 3);
    f32x16 oa, ob;
    {
        int cnt = 0;
        const int pub = __builtin_amdgcn_readfirstlane(w == 0 ? 1 : 0);
        unsigned* yflag = a.flags + 256 + b;
        f32x16 sx, sy; u32x4 km0, km1, v00, v01, v1_, p0, p1; u32x2 kt0, kt1;
        #define ATT8_CLOB "memory", "scc", "s94", "s95", "s96", "s97", "v102", "v103"
        asm volatile(ATT8_A
            : "={v[0:15]}"(oa), "={v[16:31]}"(ob), "={v[32:47]}"(sx), "={v[48:63]}"(sy), "={v[76:79]}"(km0), "={v[80:81]}"(kt0), "={v[82:85]}"(v00), "={v[86:89]}"(v1_),
              "={v[90:93]}"(p0), "={v[94:97]}"(p1), "={v[106:109]}"(km1), "={v[110:111]}"(kt1), "={v[112:115]}"(v01),
              "+{v98}"(ak), "+{v99}"(at), "+{v100}"(av), [so] "+&s"(so), [cnt] "+&s"(cnt)
            : ATT_REGS_Q, "{v101}"(vd), [pub] "s"(pub), [flag] "s"(yflag), [tag] "s"(tag), [srd] "s"(srd), [ldsb] "s"(lds0)
            : ATT8_CLOB);
        if (kpart == 0) {
            poll32(a.flags + 256 + 32 * cb, lane, tag);
            const unsigned yb = (unsigned)(72 * cb) * YT_ROW + tb * 128 + w * 32;
            asm volatile("global_load_dwordx4 %0, %3, %6 sc1\n\tglobal_load_dwordx4 %1, %4, %6 sc1\n\tglobal_load_dwordx4 %2, %5, %6 sc1\n\ts_waitcnt vmcnt(0)"
                         : "=&v"(yv[0]), "=&v"(yv[1]), "=&v"(yv[2])
                         : "v"(yb + (lane >> 1) * YT_ROW + (lane & 1) * 16), "v"(yb + (32 + (lane >> 1)) * YT_ROW + (lane & 1) * 16), "v"(yb + (i2 >> 1) * YT_ROW + (i2 & 1) * 16), "s"(a.yt) : "memory");
        }
        asm volatile(ATT8_B
            : "+{v[0:15]}"(oa), "+{v[16:31]}"(ob), "+{v[32:47]}"(sx), "+{v[48:63]}"(sy), "+{v[76:79]}"(km0), "+{v[80:81]}"(kt0), "+{v[82:85]}"(v00), "+{v[86:89]}"(v1_),
              "+{v[90:93]}"(p0), "+{v[94:97]}"(p1), "+{v[106:109]}"(km1), "+{v[110:111]}"(kt1), "+{v[112:115]}"(v01),
              "+{v98}"(ak), "+{v99}"(at), "+{v100}"(av), [cnt] "+&s"(cnt), [wrow] "+&s"(wrow)
            : ATT_REGS_Q, "{v104}"(vw), [wlds] "s"(wlds), [wsl] "s"((unsigned)(uintptr_t)wsrc), [wsh] "s"((unsigned)((uintptr_t)wsrc >> 32))
            : ATT8_CLOB);
    }
    {
        const int r0 = __builtin_amdgcn_readfirstlane(7 * (w & 3) + 280 < 281 ? 280 : 281);
        const char* ws_ = wsrc + r0 * 1152; const unsigned wl_ = wlds + r0 * 144;
        if (lane < 63) glds16s(vw, ws_, wl_);
    }
    LDS char* red = sm + (qp_i < 2 ? 41472 + qp_i * 6144 : 121600 + (qp_i - 2) * 6144) + lane * 16;
    if (kpart == 0) {
        #pragma unroll
        for (int i = 0; i < 3; i++) {
            *(LDS f32x4*)(red + i * 1024) = (f32x4){oa[4 * i], oa[4 * i + 1], oa[4 * i + 2], oa[4 * i + 3]};
            *(LDS f32x4*)(red + 3072 + i * 1024) = (f32x4){ob[4 * i], ob[4 * i + 1], ob[4 * i + 2], ob[4 * i + 3]};
        }
    }
    asm volatile("s_waitcnt lgkmcnt(0)\n\ts_barrier" ::: "memory");
    if (kpart == 1) {
        finish_tiles(a.vimg, a.oimg, oa, ob, red, head, qt, lane, qam, qat, qbm, qbt);
    } else {
        LDS char* ly = sm + 59904 + w * 2688;
        #pragma unroll
        for (int z = 0; z < 3; z++) {
            const int i = z == 0 ? lane : z == 1 ? lane + 64 : i2;
            LDS unsigned* d = (LDS unsigned*)(ly + (i >> 1) * 36 + (i & 1) * 16);
            d[0] = yv[z][0]; d[1] = yv[z][1]; d[2] = yv[z][2]; d[3] = yv[z][3];
        }
        const int c = lane % 9;
        #pragma unroll
        for (int z = 0; z < 3; z++) {
            const int t = 7 * z + lane / 9;
            u32x4 r;
            #pragma unroll
            for (int e = 0; e < 4; e++) {
                const int tc = t < 16 ? t : 15;
                const float ya = bf2f(*(LDS u16*)(ly + (c * 8 + 2 * e) * 36 + tc * 2)), yb_ = bf2f(*(LDS u16*)(ly + (c * 8 + 2 * e + 1) * 36 + tc * 2));
                const unsigned sk = pk(__uint_as_float(uv[z][e] << 16) * __uint_as_float(dv0[e] << 16), __uint_as_float(uv[z][e] & 0xffff0000u) * __uint_as_float(dv0[e] & 0xffff0000u));
                r[e] = pk(ya + __uint_as_float(sk << 16), yb_ + __uint_as_float(sk & 0xffff0000u));
            }
            if (lane < 63 && t < 16) st16_sc1((char*)a.out, (unsigned)(trow + t) * 2304 + cb * 144 + c * 16, r);
        }
    }
    asm volatile("s_waitcnt vmcnt(0)\n\ts_barrier" ::: "memory");
    if (w == 0 && lane < 8) __hip_atomic_store(a.flags + 1024 + lane * 256 + b, tag, __ATOMIC_RELAXED, __HIP_MEMORY_SCOPE_AGENT);
    {
        if (w == 0) poll32(a.flags + 1024 + (b >> 5) * 256 + 32 * (tb >> 2), lane, tag);
        asm volatile("s_barrier" ::: "memory");
        const int t4 = w & 3;
        const int g4 = lane >> 4, q4 = (lane & 15) >> 2, p4 = lane & 3;
        LDS char* wa = sm + (18 * (g4 >> 1) + 4 * (g4 & 1) + q4) * 144 + p4 * 8;
        LDS char* wt = sm + (18 * (4 * g4 + (q4 >> 1)) + 16 + (q4 & 1)) * 144 + p4 * 8;
        f32x4 acc[5];
        #pragma unroll
        for (int j = 0; j < 5; j++) acc[j] = (f32x4){0.f, 0.f, 0.f, 0.f};
        const unsigned go = (unsigned)(tb * 4 + t4) * OT_B + lane * 16;
        const unsigned gt = OTAIL + (4 * g4) * 8192 + (tb * 64 + t4 * 16 + (lane & 15)) * 4;
        if (w < 4) oproj_half<0>(acc, a.oimg, go, gt, wa, wt); else oproj_half<1>(acc, a.oimg, go, gt, wa, wt);
        LDS char* ex = sm + 41472 + t4 * 5120 + lane * 16;
        if (w >= 4) {
            #pragma unroll
            for (int j = 0; j < 5; j++) *(LDS f32x4*)(ex + j * 1024) = acc[j];
        }
        asm volatile("s_waitcnt lgkmcnt(0)\n\ts_barrier" ::: "memory");
        LDS char* st = sm + 65536;
        if (w < 4) {
            #pragma unroll
            for (int j = 0; j < 5; j++) {
                const f32x4 o2 = acc[j] + *(LDS f32x4*)(ex + j * 1024);
                const int n = j * 16 + 4 * g4;
                if (n < 72) *(LDS u32x2*)(st + (t4 * 16 + (lane & 15)) * 144 + n * 2) = (u32x2){pk(o2[0], o2[1]), pk(o2[2], o2[3])};
            }
            asm volatile("s_waitcnt lgkmcnt(0)" ::: "memory");
            #pragma unroll
            for (int z = 0; z < 3; z++) {
                const int t = 7 * z + lane / 9, c = lane % 9;
                if (lane < 63 && t < 16) st16_sc1((char*)a.out, (unsigned)(tb * 64 + t4 * 16 + t) * 2304 + 1152 + cb * 144 + c * 16, *(LDS u32x4*)(st + (t4 * 16 + t) * 144 + c * 16));
            }
        }
    }
}
extern "C" void kernel_launch(void* const* d_in, const int* in_sizes, int n_in,
                              void* d_out, int out_size, void* d_ws, size_t ws_size,
                              hipStream_t stream) {
    const u16* u = (const u16*)d_in[0];
    const u16* Are = (const u16*)d_in[1]; const u16* Aim = (const u16*)d_in[2];
    const u16* Bre = (const u16*)d_in[3]; const u16* Bim = (const u16*)d_in[4];
    const u16* Cre = (const u16*)d_in[5]; const u16* Cim = (const u16*)d_in[6];
    const u16* logdt = (const u16*)d_in[7]; const u16* Dskip = (const u16*)d_in[8];
    const u16* x = (const u16*)d_in[9]; const u16* wqkv = (const u16*)d_in[10]; const u16* wo = (const u16*)d_in[11];
    char* ws = (char*)d_ws;
    char* vimg = ws;
    char* oimg = ws + (12 << 20);
    char* ut = ws + (16 << 20);
    char* yt = ws + (20 << 20);
    unsigned* flags = (unsigned*)(ws + (24 << 20));
    (void)hipFuncSetAttribute((const void*)k1_kernel, hipFuncAttributeMaxDynamicSharedMemorySize, K1_LDS);
    (void)hipFuncSetAttribute((const void*)k2_kernel, hipFuncAttributeMaxDynamicSharedMemorySize, K2_LDS);
    char* kimg = ws + (23 << 20);
    K1Args a1 = {x, wqkv, u, Are, Aim, Bre, Bim, Cre, Cim, logdt, vimg, ut, kimg, flags, wo};
    k1_kernel<<<256, 512, K1_LDS, stream>>>(a1);
    K2Args a2 = {vimg, ut, kimg, Are, Aim, Bre, Bim, Cre, Cim, logdt, u, Dskip, wo, yt, oimg, flags, (u16*)d_out};
    k2_kernel<<<256, 512, K2_LDS, stream>>>(a2);
}
```
